```python
import torch

def _softmax(x, axis=-1):
    return torch.softmax(x, dim=axis)
def _gelu(x):
    return torch.nn.functional.gelu(x, approximate="tanh")
def _silu(x):
    return torch.nn.functional.silu(x)
def _sigmoid(x):
    return torch.sigmoid(x)
def _layernorm(x, axis=-1, eps=1e-5):
    m = x.mean(dim=axis, keepdim=True)
    v = x.var(dim=axis, keepdim=True, unbiased=False)
    return (x - m) / torch.sqrt(v + eps)
def _scatter_add(tgt, idx, src, axis=0):
    return tgt.clone().index_add_(axis, idx, src)
def _segment_sum(src, seg, n):
    out = torch.zeros((n,) + src.shape[1:], dtype=src.dtype)
    return out.index_add_(0, seg, src)
def _pad(x, pw):
    flat = tuple(v for pair in reversed(pw) for v in pair)
    return torch.nn.functional.pad(x, flat)

kernel_name = 'parallel_sum__linear_kernel_hedgehog__glu_gated_reglu__prompt'

def setup_inputs():

    _w = (lambda a: (lambda t: t.to(torch.bfloat16) if t.is_floating_point() else t)(torch.as_tensor(a)))
    def _A_setup():
        B, S, D, H = 1, 2048, 448, 16
        Dh = D // H
        return {
            "x": _w((lambda *s: torch.randn(*s, dtype=torch.bfloat16) * 0.05)(B, S, D)),
            "wqkv": _w((lambda *s: torch.randn(*s, dtype=torch.bfloat16) * 0.2)(D, 3 * D)),
            "w_phi": _w((lambda *s: torch.randn(*s, dtype=torch.bfloat16) * 0.2)(H, Dh, Dh) * (Dh ** -0.5)),
            "wo": _w((lambda *s: torch.randn(*s, dtype=torch.bfloat16) * 0.05)(D, D)),
        }
    _A_in = _A_setup()

    def _B_setup():
        B, S, D, F = 1, 2048, 448, 1792
        return {
            "x": _w((lambda *s: torch.randn(*s, dtype=torch.bfloat16) * 0.05)(B, S, D)),
            "W_gate": _w((lambda *s: torch.randn(*s, dtype=torch.bfloat16) * 0.4)(D, F) * (2.0 / D) ** 0.5),
            "W_up": _w((lambda *s: torch.randn(*s, dtype=torch.bfloat16) * 0.4)(D, F) * (2.0 / D) ** 0.5),
            "W_down": _w((lambda *s: torch.randn(*s, dtype=torch.bfloat16) * 0.4)(F, D) * (1.0 / F) ** 0.5),
        }
    _B_in = _B_setup()

    return {
        "A_x": _A_in["x"],
        "A_wqkv": _A_in["wqkv"],
        "A_w_phi": _A_in["w_phi"],
        "A_wo": _A_in["wo"],
        "B_x": _B_in["x"],
        "B_W_gate": _B_in["W_gate"],
        "B_W_up": _B_in["W_up"],
        "B_W_down": _B_in["W_down"],
    }

def reference(A_x, A_wqkv, A_w_phi, A_wo, B_x, B_W_gate, B_W_up, B_W_down):
    def _A_ref(x, wqkv, w_phi, wo):
        B, S, D = x.shape
        H = 16; Dh = D // H
        qkv = (lambda x, *d: x.permute(*d))((x @ wqkv).reshape(B, S, 3, H, Dh), 2, 0, 3, 1, 4)
        q, k, v = qkv[0], qkv[1], qkv[2]
        pq = torch.einsum("bhsd,hdf->bhsf", q, w_phi)
        pk = torch.einsum("bhsd,hdf->bhsf", k, w_phi)
        phi_q = torch.concatenate([_softmax(pq, axis=-1), _softmax(-pq, axis=-1)], axis=-1)
        phi_k = torch.concatenate([_softmax(pk, axis=-1), _softmax(-pk, axis=-1)], axis=-1)
        kv = torch.einsum("bhsf,bhsd->bhfd", phi_k, v)
        z  = torch.einsum("bhsf->bhf", phi_k)
        num = torch.einsum("bhqf,bhfd->bhqd", phi_q, kv)
        den = torch.einsum("bhqf,bhf->bhq", phi_q, z)[..., None] + 1e-6
        o = num / den
        return (lambda x, *d: x.permute(*d))(o, 0, 2, 1, 3).reshape(B, S, D) @ wo

    def _B_ref(x, W_gate, W_up, W_down):
        gate = torch.einsum("bsd,df->bsf", x, W_gate)
        up = torch.einsum("bsd,df->bsf", x, W_up)
        gate = gate * (lambda x, dt: x.to(dt))(gate > 0, gate.dtype)
        h = gate * up
        return torch.einsum("bsf,fd->bsd", h, W_down)

    x = A_x
    xn = _layernorm(x, axis=-1)
    return x + _A_ref(xn, A_wqkv, A_w_phi, A_wo) + _B_ref(xn, B_W_gate, B_W_up, B_W_down)
```

```cpp
#include <hip/hip_runtime.h>
#include <hip/hip_bf16.h>
#include <stdint.h>

typedef unsigned short u16;
__device__ __forceinline__ float bf2f(u16 b) { return __uint_as_float(((unsigned)b) << 16); }
__device__ __forceinline__ u16 f2bf(float f) { unsigned u = __float_as_uint(f); u += 0x7FFFu + ((u >> 16) & 1u); return (u16)(u >> 16); }
__device__ __forceinline__ float rbf(float f) { return bf2f(f2bf(f)); }

constexpr int S = 2048, D = 448, H = 16, DH = 28, F = 1792;

__global__ void k_ln(const u16* x, u16* xn) {
  int r = blockIdx.x, t = threadIdx.x;
  float v[7]; float s = 0;
  for (int i = 0; i < 7; ++i) { v[i] = bf2f(x[r * D + i * 64 + t]); s += v[i]; }
  for (int o = 32; o; o >>= 1) s += __shfl_xor(s, o);
  float m = s / D; float q = 0;
  for (int i = 0; i < 7; ++i) { float d = v[i] - m; q += d * d; }
  for (int o = 32; o; o >>= 1) q += __shfl_xor(q, o);
  float var = rbf(q / D); float mb = rbf(m);
  float sd = rbf(sqrtf(rbf(var + 1e-5f)));
  for (int i = 0; i < 7; ++i) xn[r * D + i * 64 + t] = f2bf(rbf(v[i] - mb) / sd);
}
__global__ void k_gemm(const u16* A, const u16* B, u16* C, int M, int N, int K) {
  int n = blockIdx.x * 64 + threadIdx.x, m0 = blockIdx.y * 8;
  if (n >= N) return;
  float acc[8] = {0};
  for (int k = 0; k < K; ++k) {
    float b = bf2f(B[k * N + n]);
#pragma unroll
    for (int i = 0; i < 8; ++i) acc[i] += bf2f(A[(m0 + i) * K + k]) * b;
  }
#pragma unroll
  for (int i = 0; i < 8; ++i) C[(m0 + i) * N + n] = f2bf(acc[i]);
}
__global__ void k_phi(const u16* qkv, const u16* wphi, u16* phiq, u16* phik) {
  int r = blockIdx.x, h = blockIdx.y, which = blockIdx.z, t = threadIdx.x;
  float p = 0;
  if (t < DH) for (int d = 0; d < DH; ++d) p += bf2f(qkv[r * 3 * D + which * D + h * DH + d]) * bf2f(wphi[(h * DH + d) * DH + t]);
  p = rbf(p);
  float mx = t < DH ? p : -1e30f, mn = t < DH ? -p : -1e30f;
  for (int o = 32; o; o >>= 1) { mx = fmaxf(mx, __shfl_xor(mx, o)); mn = fmaxf(mn, __shfl_xor(mn, o)); }
  float e1 = t < DH ? __expf(p - mx) : 0.f, e2 = t < DH ? __expf(-p - mn) : 0.f;
  float s1 = e1, s2 = e2;
  for (int o = 32; o; o >>= 1) { s1 += __shfl_xor(s1, o); s2 += __shfl_xor(s2, o); }
  u16* dst = which ? phik : phiq;
  if (t < DH) { dst[(r * H + h) * 56 + t] = f2bf(e1 / s1); dst[(r * H + h) * 56 + 28 + t] = f2bf(e2 / s2); }
}
__global__ void k_kv(const u16* qkv, const u16* phik, float* kv) {
  int h = blockIdx.x, f = blockIdx.y, d = threadIdx.x;
  if (d > DH) return;
  float a = 0;
  for (int s = 0; s < S; ++s) a += bf2f(phik[(s * H + h) * 56 + f]) * (d < DH ? bf2f(qkv[s * 3 * D + 2 * D + h * DH + d]) : 1.f);
  kv[(h * 56 + f) * 32 + d] = rbf(a);
}
__global__ void k_o(const u16* phiq, const float* kv, u16* o) {
  int r = blockIdx.x, h = blockIdx.y, d = threadIdx.x;
  if (d >= DH) return;
  float num = 0, den = 0;
  for (int f = 0; f < 56; ++f) { float p = bf2f(phiq[(r * H + h) * 56 + f]); num += p * kv[(h * 56 + f) * 32 + d]; den += p * kv[(h * 56 + f) * 32 + 28]; }
  o[r * D + h * DH + d] = f2bf(rbf(num) / rbf(rbf(den) + 1e-6f));
}
__global__ void k_h(const u16* g, const u16* u, u16* h, int n) {
  int i = blockIdx.x * 256 + threadIdx.x; if (i >= n) return;
  float gv = bf2f(g[i]); gv = gv > 0 ? gv : gv * 0.f;
  h[i] = f2bf(gv * bf2f(u[i]));
}
__global__ void k_fin(const u16* x, const u16* a, const u16* b, u16* out, int n) {
  int i = blockIdx.x * 256 + threadIdx.x; if (i >= n) return;
  out[i] = f2bf(rbf(bf2f(x[i]) + bf2f(a[i])) + bf2f(b[i]));
}

extern "C" void kernel_launch(void* const* d_in, const int* in_sizes, int n_in, void* d_out, int out_size, void* d_ws, size_t ws_size, hipStream_t stream) {
  const u16* x = (const u16*)d_in[0]; const u16* wqkv = (const u16*)d_in[1]; const u16* wphi = (const u16*)d_in[2]; const u16* wo = (const u16*)d_in[3];
  const u16* wg = (const u16*)d_in[5]; const u16* wu = (const u16*)d_in[6]; const u16* wd = (const u16*)d_in[7];
  char* ws = (char*)d_ws; size_t off = 0;
  auto alloc = [&](size_t b) { char* p = ws + off; off += (b + 255) & ~(size_t)255; return p; };
  u16* xn = (u16*)alloc(S * D * 2); u16* qkv = (u16*)alloc(S * 3 * D * 2); u16* phiq = (u16*)alloc(S * H * 56 * 2); u16* phik = (u16*)alloc(S * H * 56 * 2);
  float* kv = (float*)alloc(H * 56 * 32 * 4); u16* o = (u16*)alloc(S * D * 2); u16* a = (u16*)alloc(S * D * 2);
  u16* g = (u16*)alloc(S * F * 2); u16* u = (u16*)alloc(S * F * 2); u16* hh = (u16*)alloc(S * F * 2); u16* b = (u16*)alloc(S * D * 2);
  k_ln<<<S, 64, 0, stream>>>(x, xn);
  k_gemm<<<dim3(21, S / 8), 64, 0, stream>>>(xn, wqkv, qkv, S, 3 * D, D);
  k_phi<<<dim3(S, H, 2), 64, 0, stream>>>(qkv, wphi, phiq, phik);
  k_kv<<<dim3(H, 56), 64, 0, stream>>>(qkv, phik, kv);
  k_o<<<dim3(S, H), 64, 0, stream>>>(phiq, kv, o);
  k_gemm<<<dim3(7, S / 8), 64, 0, stream>>>(o, wo, a, S, D, D);
  k_gemm<<<dim3(28, S / 8), 64, 0, stream>>>(xn, wg, g, S, F, D);
  k_gemm<<<dim3(28, S / 8), 64, 0, stream>>>(xn, wu, u, S, F, D);
  k_h<<<(S * F + 255) / 256, 256, 0, stream>>>(g, u, hh, S * F);
  k_gemm<<<dim3(7, S / 8), 64, 0, stream>>>(hh, wd, b, S, D, F);
  k_fin<<<(S * D + 255) / 256, 256, 0, stream>>>(x, a, b, (u16*)d_out, S * D);
}
```

```cpp
#include <hip/hip_runtime.h>
#include <stdint.h>

typedef unsigned u32;
typedef unsigned long long u64;
typedef short bf16x8 __attribute__((ext_vector_type(8)));
typedef short s16x4 __attribute__((ext_vector_type(4)));
typedef float f32x4 __attribute__((ext_vector_type(4)));
typedef float f32x2 __attribute__((ext_vector_type(2)));
typedef u32 u32x4 __attribute__((ext_vector_type(4)));
typedef u32 u32x2 __attribute__((ext_vector_type(2)));
typedef __bf16 bf16x2 __attribute__((ext_vector_type(2)));
typedef __amdgpu_buffer_rsrc_t srd_t;
#define LAS __attribute__((address_space(3)))
#define GAS __attribute__((address_space(1)))
#define DI __device__ __forceinline__

constexpr int S_ = 2048, D_ = 448, F_ = 1792;
constexpr u32 XS = 896, QS = 2688, GS = 3584;
constexpr u32 SLOT = 16384;
constexpr u32 WS_EPOCH = 0, WS_FP = 1024, WS_FR = 3072, WS_FH = 4096, WS_FO = 8192, WS_KVF = 16384,
              WS_PART = 131072, WS_HB = 4194304, WS_END = WS_HB + 128u * 70u * 1024u, WS_DBG = 16u << 20;
constexpr int SPIN_CAP = 1 << 15;

#define STAMP(i) do {} while (0)
#define STAMP_DECL
#define STAMP_FLUSH() do {} while (0)

DI u32 rfl(u32 x) { return __builtin_amdgcn_readfirstlane(x); }
DI srd_t mk_srd(const void* p, u32 bytes) { return __builtin_amdgcn_make_buffer_rsrc((void*)p, (short)0, (int)bytes, 0x00020000); }
DI void dma16(u32 m0v, u32 voff, srd_t srd, u32 soff) {
  asm volatile("s_mov_b32 m0, %0\n\ts_nop 0\n\tbuffer_load_dwordx4 %1, %2, %3 offen lds" :: "s"(m0v), "v"(voff), "s"(srd), "s"(soff) : "memory");
}
#define WAITV(n) asm volatile("s_waitcnt vmcnt(" #n ")" ::: "memory")
#define BARRIER() asm volatile("s_waitcnt lgkmcnt(0)\n\ts_barrier" ::: "memory")
DI f32x4 mfma(bf16x8 a, bf16x8 b, f32x4 c) { return __builtin_amdgcn_mfma_f32_16x16x32_bf16(a, b, c, 0, 0, 0); }
DI bf16x8 trfrag(LAS char* p0, LAS char* p1) {
  s16x4 lo = __builtin_amdgcn_ds_read_tr16_b64_v4i16((LAS s16x4*)p0);
  s16x4 hi = __builtin_amdgcn_ds_read_tr16_b64_v4i16((LAS s16x4*)p1);
  return __builtin_shufflevector(lo, hi, 0, 1, 2, 3, 4, 5, 6, 7);
}
DI u32 pk2(float a, float b) { f32x2 f = {a, b}; return __builtin_bit_cast(u32, __builtin_convertvector(f, bf16x2)); }
DI float rbf(float a) { return __uint_as_float(pk2(a, a) << 16); }
DI float dot2(u32 a, u32 b, float c) { return __builtin_amdgcn_fdot2_f32_bf16(__builtin_bit_cast(bf16x2, a), __builtin_bit_cast(bf16x2, b), c, false); }
DI bf16x8 as_frag(u32x4 v) { return __builtin_bit_cast(bf16x8, v); }
#define ROWRED(x, OP) do { auto a_ = __builtin_amdgcn_permlane16_swap(__float_as_uint(x), __float_as_uint(x), false, false); \
    x = OP(__uint_as_float(a_[0]), __uint_as_float(a_[1])); \
    auto b_ = __builtin_amdgcn_permlane32_swap(__float_as_uint(x), __float_as_uint(x), false, false); \
    x = OP(__uint_as_float(b_[0]), __uint_as_float(b_[1])); } while (0)
DI float fadd_(float a, float b) { return a + b; }
DI float fmax_(float a, float b) { return __builtin_fmaxf(a, b); }
DI float fmin_(float a, float b) { return __builtin_fminf(a, b); }

DI u32x4 center(u32x4 x, float nmb) {
  u32x4 r; u32 c0 = 0x00003f80u, c1 = 0x3f800000u;
  asm("" : "+s"(c0), "+s"(c1));
#pragma unroll
  for (int i = 0; i < 4; ++i) r[i] = pk2(dot2(x[i], c0, nmb), dot2(x[i], c1, nmb));
  return r;
}

DI void dual_softmax(f32x4 p0, f32x4 p1, float w1, u32x4& fp, u32x4& fm) {
  float mx = fmax_(fmax_(fmax_(p0[0], p0[1]), fmax_(p0[2], p0[3])), fmax_(fmax_(p1[0], p1[1]), fmax_(p1[2], p1[3])));
  float mn = fmin_(fmin_(fmin_(p0[0], p0[1]), fmin_(p0[2], p0[3])), fmin_(fmin_(p1[0], p1[1]), fmin_(p1[2], p1[3])));
  ROWRED(mx, fmax_); ROWRED(mn, fmin_);
  const float L = 1.4426950408889634f;
  const float a = -mx * L, b = mn * L;
  float ep[8], em[8];
#pragma unroll
  for (int i = 0; i < 4; ++i) {
    ep[i] = __builtin_amdgcn_exp2f(__builtin_fmaf(p0[i], L, a)); ep[4 + i] = __builtin_amdgcn_exp2f(__builtin_fmaf(p1[i], L, a));
    em[i] = __builtin_amdgcn_exp2f(__builtin_fmaf(p0[i], -L, b)); em[4 + i] = __builtin_amdgcn_exp2f(__builtin_fmaf(p1[i], -L, b));
  }
  float sp = __builtin_fmaf((ep[4] + ep[5]) + (ep[6] + ep[7]), w1, (ep[0] + ep[1]) + (ep[2] + ep[3]));
  float sm = __builtin_fmaf((em[4] + em[5]) + (em[6] + em[7]), w1, (em[0] + em[1]) + (em[2] + em[3]));
  ROWRED(sp, fadd_); ROWRED(sm, fadd_);
  const float ip = __builtin_amdgcn_rcpf(sp), im = __builtin_amdgcn_rcpf(sm), ip1 = ip * w1, im1 = im * w1;
  fp[0] = pk2(ep[0] * ip, ep[1] * ip); fp[1] = pk2(ep[2] * ip, ep[3] * ip); fp[2] = pk2(ep[4] * ip1, ep[5] * ip1); fp[3] = pk2(ep[6] * ip1, ep[7] * ip1);
  fm[0] = pk2(em[0] * im, em[1] * im); fm[1] = pk2(em[2] * im, em[3] * im); fm[2] = pk2(em[4] * im1, em[5] * im1); fm[3] = pk2(em[6] * im1, em[7] * im1);
}
DI u32x4 scale_pack(f32x4 a, f32x4 b, float s) {
  u32x4 r; r[0] = pk2(a[0] * s, a[1] * s); r[1] = pk2(a[2] * s, a[3] * s); r[2] = pk2(b[0] * s, b[1] * s); r[3] = pk2(b[2] * s, b[3] * s); return r;
}
DI void wait_flags(const char* ws, u32 off, int n, u64 epoch, int lane) {
  const GAS u64* p = (const GAS u64*)(ws + off) + (lane < n ? lane : n - 1);
#pragma unroll 1
  for (int spin = 0; spin < SPIN_CAP; ++spin) {
    const u64 v = __hip_atomic_load(p, __ATOMIC_RELAXED, __HIP_MEMORY_SCOPE_AGENT);
    if (__all(v == epoch)) break;
    __builtin_amdgcn_s_sleep(2);
  }
  __builtin_amdgcn_fence(__ATOMIC_ACQUIRE, "wavefront");
}
DI void set_flag(char* ws, u32 off, u64 epoch) { __hip_atomic_store((GAS u64*)(ws + off), epoch, __ATOMIC_RELAXED, __HIP_MEMORY_SCOPE_AGENT); }

template <bool Q>
DI void body(const char* __restrict__ x, const char* __restrict__ wqkv, const char* __restrict__ wphi,
    const char* __restrict__ wo, const char* __restrict__ wg, const char* __restrict__ wu, const char* __restrict__ wd, char* __restrict__ out, char* ws) {
  extern __shared__ __attribute__((aligned(16))) char smem_[];
  LAS char* lds = (LAS char*)smem_;
  const u32 ldsb = (u32)(size_t)lds;
  const int tid = threadIdx.x, lane = tid & 63;
  const u32 w = rfl(tid >> 6), m = w & 3, n = w >> 2;
  const u32 c16 = lane & 15, g = lane >> 4, rho = lane >> 3, cc = lane & 7, q4 = c16 >> 2, p4 = lane & 3;
  const u32 b = blockIdx.x, xcd = b & 7, idx = b >> 3, pair = xcd >> 1, ch = xcd & 1;
  const u32 rg = 4 * pair + (idx >> 3), jj = Q ? 7 : (idx & 7);
  const u32 prod = 7 * ch + jj;
  u32 head = 8 * ch + jj;
  if (ch == 0) { if (jj == 6) head = 7; if (jj == 7) head = 6; } else { if (jj == 1) head = 15; if (jj == 7) head = 9; }
  const u32 rh = 8 * pair + 4 * ch + (idx >> 3);
  STAMP_DECL
  STAMP(0);
  const u64 epoch = *(const __attribute__((address_space(4))) u64*)(ws + WS_EPOCH) + 0x9E3779B97F4A7C15ull;

  const srd_t sX = mk_srd(x, S_ * XS), sQ = mk_srd(wqkv, D_ * QS), sP = mk_srd(wphi, 16 * 1568), sW = mk_srd(ws, WS_END);
  const srd_t sU0 = mk_srd(Q ? wqkv : wg, Q ? D_ * QS : D_ * GS), sU1 = mk_srd(Q ? wqkv : wu, Q ? D_ * QS : D_ * GS);
  const srd_t sD = mk_srd(wd, F_ * XS), sO = mk_srd(wo, D_ * XS);

  {
    const u32 vx0 = (16 * w + rho) * XS + 16 * (cc ^ (rho >> 1)), vx1 = (16 * w + 8 + rho) * XS + 16 * (cc ^ (4 + (rho >> 1)));
#pragma unroll
    for (int j = 0; j < 7; ++j) {
      dma16(ldsb + j * SLOT + w * 2048, vx0, sX, rg * 128 * XS + 128 * j);
      dma16(ldsb + j * SLOT + w * 2048 + 1024, vx1, sX, rg * 128 * XS + 128 * j);
    }
  }
  const u32 hwin = (56 * head) & ~15u;
  auto kvoff = [&](u32 row, u32 sg) __attribute__((always_inline)) { const u32 lc = cc ^ (sg << 1); return row * QS + 896 + hwin + (lc < 4 ? 16 * lc : 896 + 16 * (lc - 4)); };
  {
    const u32 vk0 = kvoff(16 * w + rho, (rho >> 1) & 1), vk1 = kvoff(16 * w + 8 + rho, ((rho >> 1) & 1) | 2);
#pragma unroll
    for (int i = 0; i < 3; ++i) {
      dma16(ldsb + (7 + i) * SLOT + w * 2048, vk0, sQ, 128 * i * QS);
      dma16(ldsb + (7 + i) * SLOT + w * 2048 + 1024, vk1, sQ, 128 * i * QS);
    }
  }
  asm volatile("" :: "s"(epoch));

  const u32 sgU = ((rho >> 1) & 1) | ((w & 1) << 1), lcU = cc ^ (sgU << 1);
  const u32 vU = Q ? (8 * w + rho) * QS + (lcU < 4 ? 16 * lcU : 16 * lcU - 16) : (8 * w + rho) * GS + 16 * lcU;
  const u32 strU = Q ? 64 * QS : 64 * GS;
  const u32 sofU0 = Q ? 56 * (8 * ch) : 256 * prod, sofU1 = Q ? 56 * (8 * ch + 2) : 256 * prod, sofUu = Q ? 224 : 128;
  const u32 vD = rho * XS + 16 * (cc ^ (((rho >> 1) & 3) << 1));
  auto issueU = [&](int u, int c, int slot) __attribute__((always_inline)) {
    dma16(ldsb + slot * SLOT + w * 1024, vU, sU0, sofU0 + u * sofUu + c * strU);
    dma16(ldsb + slot * SLOT + 8192 + w * 1024, vU, sU1, sofU1 + u * sofUu + c * strU);
  };
  auto issueD = [&](int d) __attribute__((always_inline)) {
    const int i = d >> 1, slot = (25 + d) % 10;
    const u32 kap = w + 8 * i;
    u32 row;
    if (i <= 6) { const u32 u = kap >= 28 ? 1 : 0, r = kap - 28 * u; row = 128 * (r >> 1) + 64 * u + 32 * (r & 1); } else row = (kap - 56) * 32;
    const u32 so = (row + 16 * (d & 1)) * XS + 128 * jj;
    dma16(ldsb + slot * SLOT + w * 2048, vD, i <= 6 ? sD : sO, so);
    dma16(ldsb + slot * SLOT + w * 2048 + 1024, vD, i <= 6 ? sD : sO, so + 8 * XS);
  };
  auto issue = [&](int t) __attribute__((always_inline)) {
    if (t == 10) {
      dma16(ldsb + w * 1024, kvoff(8 * w + rho, sgU), sQ, 384 * QS);
      dma16(ldsb + 8192 + (w & 1) * 1024, lane * 16, sP, head * 1568 + (w & 1) * 1024);
    } else if (t <= 17) issueU(0, t - 11, t % 10);
    else if (t <= 24) issueU(1, t - 18, t % 10);
  };

  u32x4 xa[14], xb[14];
  float s1 = 0.f, s2 = 0.f;
  {
    const u32 xr = (32 * m + 16 * n + c16) * 128 + 16 * (g ^ (c16 >> 1));
#pragma unroll
    for (int j = 0; j < 7; ++j) {
      if (j == 0) WAITV(18); else WAITV(16);
      BARRIER();
      if (j >= 1) issue(j + 9);
      LAS char* sl = lds + j * SLOT;
      xa[2 * j] = *(LAS u32x4*)(sl + xr); xa[2 * j + 1] = *(LAS u32x4*)(sl + (xr ^ 64));
      xb[2 * j] = *(LAS u32x4*)(sl + (xr ^ 2048)); xb[2 * j + 1] = *(LAS u32x4*)(sl + (xr ^ 2048 ^ 64));
#pragma unroll
      for (int i = 0; i < 4; ++i) {
        s1 = dot2(xa[2 * j][i], 0x3f803f80u, s1); s2 = dot2(xa[2 * j][i], xa[2 * j][i], s2);
        s1 = dot2(xa[2 * j + 1][i], 0x3f803f80u, s1); s2 = dot2(xa[2 * j + 1][i], xa[2 * j + 1][i], s2);
      }
    }
  }
  STAMP(1);
  ROWRED(s1, fadd_); ROWRED(s2, fadd_);
  float nma, rsa;
  {
    const float mean = s1 * (1.f / 448.f);
    const float var = fmax_(__builtin_fmaf(-mean, mean, s2 * (1.f / 448.f)), 0.f);
    nma = -rbf(mean);
    rsa = 1.f / rbf(__builtin_sqrtf(rbf(rbf(var) + 1e-5f)));
    f32x2 st = {nma, rsa};
    if (g == 0) *(LAS f32x2*)(lds + 10240 + (32 * m + 16 * n + c16) * 8) = st;
  }
  const u32 sig = (q4 >> 1) | ((g & 1) << 1);
  const float w1 = g == 3 ? 0.f : 1.f;
  float nmb, rsb;

  f32x4 ak[4] = {};
  {
    const u32 o = (head & 1) * 8;
    u32 lb0 = o + 8 * p4, lb1 = o + 32 + 8 * p4; if (lb1 >= 64) lb1 -= 8;
    const u32 rowb = (8 * g + q4) * 128;
    const u32 r0 = rowb + ((((lb0 >> 5) ^ sig) << 5) | (lb0 & 31)), r1 = rowb + ((((lb1 >> 5) ^ sig) << 5) | (lb1 & 31));
#pragma unroll
    for (int i = 0; i < 4; ++i) {
      if (i < 3) WAITV(16); else WAITV(14);
      BARRIER();
      if (i < 2) issue(16 + i);
      if (i == 0) { const f32x2 st = *(LAS f32x2*)(lds + 10240 + (32 * m + 16 * (n ^ 1) + c16) * 8); nmb = st[0]; rsb = st[1]; }
      LAS char* sl = lds + ((7 + i) % 10) * SLOT;
#pragma unroll
      for (int s = 0; s < (i < 3 ? 4 : 2); ++s) {
        LAS char* p = sl + s * 4096;
        const bf16x8 f0 = trfrag(p + r0, p + r0 + 512), f1 = trfrag(p + r1, p + r1 + 512);
        const bf16x8 f2 = trfrag(p + (r0 ^ 64), p + (r0 ^ 64) + 512), f3 = trfrag(p + (r1 ^ 64), p + (r1 ^ 64) + 512);
        xa[4 * i + s] = center(xa[4 * i + s], nma);
        const bf16x8 xf = as_frag(xa[4 * i + s]);
        ak[0] = mfma(f0, xf, ak[0]); ak[1] = mfma(f1, xf, ak[1]); ak[2] = mfma(f2, xf, ak[2]); ak[3] = mfma(f3, xf, ak[3]);
      }
    }
  }
  STAMP(2);
  const u32 wr0 = (4 * g + q4) * 56 + 8 * p4, wr1 = (4 * g + q4) * 56 + 32 + 8 * (p4 == 3 ? 2 : p4);
  auto wphi_frag = [&](LAS char* img, u32 a) __attribute__((always_inline)) {
    u32x4 f = __builtin_bit_cast(u32x4, trfrag(img + a, img + a + 16 * 56));
    if (g == 3) { f[2] = 0; f[3] = 0; }
    return as_frag(f);
  };
  {
    const bf16x8 wf0 = wphi_frag(lds + 8192, wr0), wf1 = wphi_frag(lds + 8192, wr1);
    const bf16x8 kf = as_frag(scale_pack(ak[0], ak[1], rsa));
    const f32x4 z = {0.f, 0.f, 0.f, 0.f};
    u32x4 fp, fm;
    dual_softmax(mfma(wf0, kf, z), mfma(wf1, kf, z), w1, fp, fm);
    u32x4 vf = scale_pack(ak[2], ak[3], rsa);
    if (g == 3) { vf[2] = 0x00003f80u; vf[3] = 0; }
    const u32 tok = 32 * m + 16 * n + c16, st = ((c16 >> 1) & 1) | (((c16 >> 3) & 1) << 1);
    LAS char* pk = lds + 8 * SLOT + tok * 128 + 8 * g;
    *(LAS u32x2*)(pk + ((0 ^ st) << 5)) = u32x2{fp[0], fp[1]}; *(LAS u32x2*)(pk + ((1 ^ st) << 5)) = u32x2{fp[2], fp[3]};
    *(LAS u32x2*)(pk + ((2 ^ st) << 5)) = u32x2{fm[0], fm[1]}; *(LAS u32x2*)(pk + ((3 ^ st) << 5)) = u32x2{fm[2], fm[3]};
    LAS char* pv = lds + 9 * SLOT + tok * 64 + 8 * g;
    *(LAS u32x2*)(pv + ((0 ^ (c16 >> 3)) << 5)) = u32x2{vf[0], vf[1]}; *(LAS u32x2*)(pv + ((1 ^ (c16 >> 3)) << 5)) = u32x2{vf[2], vf[3]};
    BARRIER();
    f32x4 kv = z;
    LAS char* ra = lds + 8 * SLOT + (8 * g + q4) * 128 + (((w >> 1) ^ sig) << 5) + 8 * p4;
    LAS char* rb = lds + 9 * SLOT + (8 * g + q4) * 64 + (((w & 1) ^ (g & 1)) << 5) + 8 * p4;
#pragma unroll
    for (int s = 0; s < 4; ++s) kv = mfma(trfrag(ra + s * 4096, ra + s * 4096 + 512), trfrag(rb + s * 2048, rb + s * 2048 + 256), kv);
    __builtin_amdgcn_raw_buffer_store_b128(__builtin_bit_cast(u32x4, kv), sW, lane * 16, WS_PART + (head * 16 + rg) * 8192 + w * 1024, 16);
  }
  STAMP(3);

  u32 ra[4];
  {
    const u32 rowb = (8 * g + q4) * 128;
    if (Q) {
      u32 lb[4] = {8 * p4, 32 + 8 * p4, 72 + 8 * p4, 104 + 8 * (p4 == 3 ? 2 : p4)};
#pragma unroll
      for (int i = 0; i < 4; ++i) ra[i] = n * 8192 + rowb + ((((lb[i] >> 5) ^ sig) << 5) | (lb[i] & 31));
    } else {
      ra[0] = rowb + (((2 * n) ^ sig) << 5) + 8 * p4; ra[1] = ra[0] ^ 32; ra[2] = ra[0] + 8192; ra[3] = ra[1] + 8192;
    }
  }
  u32x4 phq[2][2][2][2];
  const u32 ttile = rg * 8 + 2 * m;
#pragma unroll
  for (int u = 0; u < 2; ++u) {
    f32x4 acc[2][4] = {};
    bf16x8 F1[4];
#pragma unroll
    for (int c = 0; c < 7; ++c) {
      const int t = 11 + 7 * u + c;
      if (t == 11) WAITV(12);
      else if (t == 12) WAITV(16);
      else if (t == 13) WAITV(8);
      else if (t == 18) { if (Q) WAITV(0); else WAITV(16); }
      else if (t == 20) { if (Q) WAITV(0); else WAITV(4); }
      else if (t > 18) { if (!Q) WAITV(16); }
      BARRIER();
      if (t == 11) { issue(18); issue(19); }
      if (t + 9 <= 24) issue(t + 9);
      else if (!Q) issueD(t + 9 - 25);
      else if (t + 9 == 25) {
        dma16(ldsb + 5 * SLOT + w * 2048, lane * 16, sP, 8 * ch * 1568 + w * 2048);
        dma16(ldsb + 5 * SLOT + w * 2048 + 1024, lane * 16, sP, 8 * ch * 1568 + w * 2048 + 1024);
      }
      if (t == 13 && tid == 0) set_flag(ws, WS_FP + (head * 16 + rg) * 8, epoch);
      if (t == 20 && tid == 0) set_flag(ws, Q ? WS_FR + (rg * 2 + ch) * 8 : WS_FH + ((rg * 2) * 16 + prod) * 8, epoch);
      LAS char* sl = lds + (t % 10) * SLOT;
      bf16x8 F0[4];
#pragma unroll
      for (int i = 0; i < 4; ++i) F0[i] = trfrag(sl + ra[i], sl + ra[i] + 512);
      if (c > 0) {
        if (u == 0) xb[2 * c - 1] = center(xb[2 * c - 1], nmb);
#pragma unroll
        for (int i = 0; i < 4; ++i) { acc[0][i] = mfma(F1[i], as_frag(xa[2 * c - 1]), acc[0][i]); acc[1][i] = mfma(F1[i], as_frag(xb[2 * c - 1]), acc[1][i]); }
      }
#pragma unroll
      for (int i = 0; i < 4; ++i) F1[i] = trfrag(sl + ra[i] + 4096, sl + ra[i] + 4096 + 512);
      if (u == 0) xb[2 * c] = center(xb[2 * c], nmb);
#pragma unroll
      for (int i = 0; i < 4; ++i) { acc[0][i] = mfma(F0[i], as_frag(xa[2 * c]), acc[0][i]); acc[1][i] = mfma(F0[i], as_frag(xb[2 * c]), acc[1][i]); }
    }
    if (u == 0) xb[13] = center(xb[13], nmb);
#pragma unroll
    for (int i = 0; i < 4; ++i) { acc[0][i] = mfma(F1[i], as_frag(xa[13]), acc[0][i]); acc[1][i] = mfma(F1[i], as_frag(xb[13]), acc[1][i]); }
    STAMP(4 + 2 * u);
    if (!Q) {
#pragma unroll
      for (int tt = 0; tt < 2; ++tt) {
        const float rs = tt ? rsb : rsa, r2 = rs * rs;
        f32x4 h0, h1;
#pragma unroll
        for (int r = 0; r < 4; ++r) { h0[r] = fmax_(acc[tt][0][r], 0.f) * acc[tt][2][r]; h1[r] = fmax_(acc[tt][1][r], 0.f) * acc[tt][3][r]; }
        __builtin_amdgcn_raw_buffer_store_b128(scale_pack(h0, h1, r2), sW, lane * 16, WS_HB + ((ttile + (n ^ tt)) * 70 + 28 * u + 2 * prod + n) * 1024, 16);
      }
    } else {
      if (u == 0) { WAITV(0); BARRIER(); }
      u32x2 part[16];
      const u32 rhead = rg, rs_ = ch;
      if (u == 0) {
        wait_flags(ws, WS_FP + rhead * 128, 16, epoch, lane);
#pragma unroll
        for (int i = 0; i < 16; ++i)
          part[i] = __builtin_amdgcn_raw_buffer_load_b64(sW, lane * 8, WS_PART + (rhead * 16 + i) * 8192 + rs_ * 4096 + w * 512, 16);
      }
      const f32x4 z = {0.f, 0.f, 0.f, 0.f};
#pragma unroll
      for (int hh = 0; hh < 2; ++hh) {
        LAS char* img = lds + 5 * SLOT + (4 * u + 2 * n + hh) * 1568;
        const bf16x8 wf0 = wphi_frag(img, wr0), wf1 = wphi_frag(img, wr1);
#pragma unroll
        for (int tt = 0; tt < 2; ++tt) {
          const bf16x8 qf = as_frag(scale_pack(acc[tt][2 * hh], acc[tt][2 * hh + 1], tt ? rsb : rsa));
          dual_softmax(mfma(wf0, qf, z), mfma(wf1, qf, z), w1, phq[u][hh][tt][0], phq[u][hh][tt][1]);
          asm volatile("" : "+v"(phq[u][hh][tt][0]), "+v"(phq[u][hh][tt][1]));
        }
      }
      if (u == 0) {
        float a0 = 0.f, a1 = 0.f;
#pragma unroll
        for (int i = 0; i < 16; ++i) { a0 += __uint_as_float(part[i][0]); a1 += __uint_as_float(part[i][1]); }
        __builtin_amdgcn_raw_buffer_store_b32(pk2(a0, a1), sW, (lane >> 1) * 16 + (lane & 1) * 4,
            WS_KVF + rhead * 4096 + (rs_ * 2 + ((w >> 1) & 1)) * 1024 + (w & 1) * 512 + (w >> 2) * 8, 16);
      }
    }
    STAMP(5 + 2 * u);
  }

  if (Q) {
    wait_flags(ws, WS_FR + ch * 128, 16, epoch, lane);
    u32x4 kf[4];
#pragma unroll
    for (int i = 0; i < 4; ++i) kf[i] = __builtin_amdgcn_raw_buffer_load_b128(sW, lane * 16, WS_KVF + (8 * ch + w) * 4096 + i * 1024, 16);
    BARRIER();
#pragma unroll
    for (int i = 0; i < 4; ++i) *(LAS u32x4*)(lds + w * 4096 + i * 1024 + lane * 16) = kf[i];
    BARRIER();
    STAMP(8);
    const f32x4 z = {0.f, 0.f, 0.f, 0.f};
#pragma unroll
    for (int u = 0; u < 2; ++u)
#pragma unroll
      for (int hh = 0; hh < 2; ++hh) {
        const u32 hl = 4 * u + 2 * n + hh;
        LAS char* kp = lds + hl * 4096 + lane * 16;
        const bf16x8 k00 = *(LAS bf16x8*)(kp), k01 = *(LAS bf16x8*)(kp + 1024), k10 = *(LAS bf16x8*)(kp + 2048), k11 = *(LAS bf16x8*)(kp + 3072);
        const u32 c0 = 28 * hl + 4 * g, c1 = c0 + 16;
        const u32 o0 = (c0 >> 5) * 1024 + (((c0 >> 2) & 3) * 16 + c16) * 16 + ((c0 >> 4) & 1) * 8;
        const u32 o1 = (c1 >> 5) * 1024 + (((c1 >> 2) & 3) * 16 + c16) * 16 + ((c1 >> 4) & 1) * 8;
#pragma unroll
        for (int tt = 0; tt < 2; ++tt) {
          const f32x4 n0 = mfma(k10, as_frag(phq[u][hh][tt][1]), mfma(k00, as_frag(phq[u][hh][tt][0]), z));
          const f32x4 n1 = mfma(k11, as_frag(phq[u][hh][tt][1]), mfma(k01, as_frag(phq[u][hh][tt][0]), z));
          auto a_ = __builtin_amdgcn_permlane16_swap(__float_as_uint(n1[0]), __float_as_uint(n1[0]), false, false);
          auto b_ = __builtin_amdgcn_permlane32_swap(a_[1], a_[1], false, false);
          const float inv = __builtin_amdgcn_rcpf(__uint_as_float(b_[1]) + 1e-6f);
          const u32x4 of = scale_pack(n0, n1, inv);
          LAS char* ob = lds + 2 * SLOT + (2 * m + (n ^ tt)) * 7168;
          *(LAS u32x2*)(ob + o0) = u32x2{of[0], of[1]};
          if (g != 3) *(LAS u32x2*)(ob + o1) = u32x2{of[2], of[3]};
        }
      }
    BARRIER();
#pragma unroll
    for (int i = 0; i < 7; ++i) {
      const u32x4 v = *(LAS u32x4*)(lds + 2 * SLOT + w * 7168 + i * 1024 + lane * 16);
      __builtin_amdgcn_raw_buffer_store_b128(v, sW, lane * 16, WS_HB + ((rg * 8 + w) * 70 + 56 + 7 * ch + i) * 1024, 16);
    }
    WAITV(0);
    BARRIER();
    if (tid == 0) set_flag(ws, WS_FO + (rg * 16 + ch) * 8, epoch);
    STAMP(9);
    STAMP_FLUSH();
    return;
  }

  WAITV(0);
  BARRIER();
  if (tid == 0) set_flag(ws, WS_FH + ((rg * 2 + 1) * 16 + prod) * 8, epoch);
  issueD(9);
  const u32 rg5 = rh >> 1;
  const u32x4 xres = __builtin_amdgcn_raw_buffer_load_b128(sX, (8 * w + rho) * XS + 16 * cc, rh * 64 * XS + 128 * jj, 0);
  f32x4 acc[4][4] = {};
  u32 rb[4];
#pragma unroll
  for (int i = 0; i < 4; ++i) rb[i] = w * 2048 + (4 * g + q4) * 128 + ((i ^ sig) << 5) + 8 * p4;
  u32x4 A[9][4];
  auto loadA = [&](int i) __attribute__((always_inline)) {
#pragma unroll
    for (int tt = 0; tt < 4; ++tt) A[i][tt] = __builtin_amdgcn_raw_buffer_load_b128(sW, lane * 16, WS_HB + ((rh * 4 + tt) * 70 + w + 8 * i) * 1024, 16);
  };
  auto kstep = [&](int i) __attribute__((always_inline)) {
    LAS char* p0 = lds + ((25 + 2 * i) % 10) * SLOT; LAS char* p1 = lds + ((26 + 2 * i) % 10) * SLOT;
    bf16x8 B[4];
#pragma unroll
    for (int ct = 0; ct < 4; ++ct) B[ct] = trfrag(p0 + rb[ct], p1 + rb[ct]);
    if (i + 5 <= 8) { asm volatile("s_waitcnt lgkmcnt(0)" : "+v"(B[0]), "+v"(B[1]), "+v"(B[2]), "+v"(B[3]) :: "memory"); issueD(2 * i + 10); issueD(2 * i + 11); }
#pragma unroll
    for (int tt = 0; tt < 4; ++tt)
#pragma unroll
      for (int ct = 0; ct < 4; ++ct) acc[tt][ct] = mfma(B[ct], as_frag(A[i][tt]), acc[tt][ct]);
  };
  wait_flags(ws, WS_FH + (rg5 * 2) * 128, 14, epoch, lane);
  STAMP(8);
  loadA(0); loadA(1); loadA(2);
  wait_flags(ws, WS_FH + (rg5 * 2 + 1) * 128, 14, epoch, lane);
  STAMP(9);
  loadA(3); loadA(4); loadA(5); loadA(6);
  kstep(0); kstep(1); kstep(2);
  wait_flags(ws, WS_FO + rg5 * 128, 2, epoch, lane);
  STAMP(10);
  loadA(7); if (w < 6) loadA(8);
  kstep(3); kstep(4); kstep(5); kstep(6);
  WAITV(0);
  kstep(7); if (w < 6) kstep(8);
  STAMP(11);
  asm volatile("s_waitcnt lgkmcnt(0)" ::: "memory");
#pragma unroll
  for (int tt = 0; tt < 4; ++tt)
#pragma unroll
    for (int ct = 0; ct < 4; ++ct)
      *(LAS f32x4*)(lds + (2 * tt + (c16 >> 3)) * SLOT + w * 2048 + (c16 & 7) * 256 + (((4 * ct + g) ^ (c16 & 7)) << 4)) = acc[tt][ct];
  BARRIER();
  f32x4 r0, r1;
#pragma unroll
  for (int i = 0; i < 4; ++i) {
    const u32 xv = xres[i];
    const float lo = __uint_as_float(xv << 16), hi = __uint_as_float(xv & 0xffff0000u);
    if (i < 2) { r0[2 * i] = lo; r0[2 * i + 1] = hi; } else { r1[2 * i - 4] = lo; r1[2 * i - 3] = hi; }
  }
#pragma unroll
  for (int j = 0; j < 8; ++j) {
    LAS char* p = lds + w * SLOT + j * 2048 + rho * 256;
    r0 += *(LAS f32x4*)(p + (((2 * cc) ^ rho) << 4)); r1 += *(LAS f32x4*)(p + (((2 * cc + 1) ^ rho) << 4));
  }
  const srd_t sOut = mk_srd(out, S_ * XS);
  __builtin_amdgcn_raw_buffer_store_b128(scale_pack(r0, r1, 1.f), sOut, (8 * w + rho) * XS + 16 * cc, rh * 64 * XS + 128 * jj, 0);
  if (b == 0 && tid == 0) *(u64*)(ws + WS_EPOCH) = epoch;
  STAMP(12);
  STAMP_FLUSH();
}

__global__ __launch_bounds__(512, 2) void fused_kernel(const char* __restrict__ x, const char* __restrict__ wqkv, const char* __restrict__ wphi,
    const char* __restrict__ wo, const char* __restrict__ wg, const char* __restrict__ wu, const char* __restrict__ wd, char* __restrict__ out, char* ws) {
  if (((blockIdx.x >> 3) & 7) == 7) body<true>(x, wqkv, wphi, wo, wg, wu, wd, out, ws);
  else body<false>(x, wqkv, wphi, wo, wg, wu, wd, out, ws);
}

extern "C" void kernel_launch(void* const* d_in, const int* in_sizes, int n_in, void* d_out, int out_size, void* d_ws, size_t ws_size, hipStream_t stream) {
  (void)in_sizes; (void)n_in; (void)out_size; (void)ws_size;
  (void)hipFuncSetAttribute((const void*)fused_kernel, hipFuncAttributeMaxDynamicSharedMemorySize, 163840);
  fused_kernel<<<256, 512, 163840, stream>>>((const char*)d_in[0], (const char*)d_in[1], (const char*)d_in[2], (const char*)d_in[3],
      (const char*)d_in[5], (const char*)d_in[6], (const char*)d_in[7], (char*)d_out, (char*)d_ws);
}
```

```cpp
#include <hip/hip_runtime.h>
#include <stdint.h>

typedef unsigned u32;
typedef unsigned long long u64;
typedef short bf16x8 __attribute__((ext_vector_type(8)));
typedef short s16x4 __attribute__((ext_vector_type(4)));
typedef float f32x4 __attribute__((ext_vector_type(4)));
typedef float f32x2 __attribute__((ext_vector_type(2)));
typedef u32 u32x4 __attribute__((ext_vector_type(4)));
typedef u32 u32x2 __attribute__((ext_vector_type(2)));
typedef __bf16 bf16x2 __attribute__((ext_vector_type(2)));
typedef __amdgpu_buffer_rsrc_t srd_t;
#define LAS __attribute__((address_space(3)))
#define GAS __attribute__((address_space(1)))
#define DI __device__ __forceinline__

constexpr int S_ = 2048, D_ = 448, F_ = 1792;
constexpr u32 XS = 896, QS = 2688, GS = 3584;
constexpr u32 SLOT = 16384;
constexpr u32 WS_EPOCH = 0, WS_FP = 1024, WS_FR = 3072, WS_FH = 4096, WS_FO = 8192, WS_KVF = 16384,
              WS_PART = 131072, WS_HB = 4194304, WS_END = WS_HB + 128u * 70u * 1024u, WS_DBG = 16u << 20;
constexpr int SPIN_CAP = 1 << 15;

#define STAMP(i) do {} while (0)
#define STAMP_DECL
#define STAMP_FLUSH() do {} while (0)

DI u32 rfl(u32 x) { return __builtin_amdgcn_readfirstlane(x); }
DI srd_t mk_srd(const void* p, u32 bytes) { return __builtin_amdgcn_make_buffer_rsrc((void*)p, (short)0, (int)bytes, 0x00020000); }
DI void dma16(u32 m0v, u32 voff, srd_t srd, u32 soff) {
  asm volatile("s_mov_b32 m0, %0\n\ts_nop 0\n\tbuffer_load_dwordx4 %1, %2, %3 offen lds" :: "s"(m0v), "v"(voff), "s"(srd), "s"(soff) : "memory");
}
#define WAITV(n) asm volatile("s_waitcnt vmcnt(" #n ")" ::: "memory")
#define BARRIER() asm volatile("s_waitcnt lgkmcnt(0)\n\ts_barrier" ::: "memory")
DI f32x4 mfma(bf16x8 a, bf16x8 b, f32x4 c) { return __builtin_amdgcn_mfma_f32_16x16x32_bf16(a, b, c, 0, 0, 0); }
DI bf16x8 trfrag(LAS char* p0, LAS char* p1) {
  s16x4 lo = __builtin_amdgcn_ds_read_tr16_b64_v4i16((LAS s16x4*)p0);
  s16x4 hi = __builtin_amdgcn_ds_read_tr16_b64_v4i16((LAS s16x4*)p1);
  return __builtin_shufflevector(lo, hi, 0, 1, 2, 3, 4, 5, 6, 7);
}
DI u32 pk2(float a, float b) { f32x2 f = {a, b}; return __builtin_bit_cast(u32, __builtin_convertvector(f, bf16x2)); }
DI float rbf(float a) { return __uint_as_float(pk2(a, a) << 16); }
DI float dot2(u32 a, u32 b, float c) { return __builtin_amdgcn_fdot2_f32_bf16(__builtin_bit_cast(bf16x2, a), __builtin_bit_cast(bf16x2, b), c, false); }
DI bf16x8 as_frag(u32x4 v) { return __builtin_bit_cast(bf16x8, v); }
#define ROWRED(x, OP) do { auto a_ = __builtin_amdgcn_permlane16_swap(__float_as_uint(x), __float_as_uint(x), false, false); \
    x = OP(__uint_as_float(a_[0]), __uint_as_float(a_[1])); \
    auto b_ = __builtin_amdgcn_permlane32_swap(__float_as_uint(x), __float_as_uint(x), false, false); \
    x = OP(__uint_as_float(b_[0]), __uint_as_float(b_[1])); } while (0)
DI float fadd_(float a, float b) { return a + b; }
DI float fmax_(float a, float b) { return __builtin_fmaxf(a, b); }
DI float fmin_(float a, float b) { return __builtin_fminf(a, b); }

DI u32x4 center(u32x4 x, float nmb) {
  u32x4 r; u32 c0 = 0x00003f80u, c1 = 0x3f800000u;
  asm("" : "+s"(c0), "+s"(c1));
#pragma unroll
  for (int i = 0; i < 4; ++i) r[i] = pk2(dot2(x[i], c0, nmb), dot2(x[i], c1, nmb));
  return r;
}

DI void dual_softmax(f32x4 p0, f32x4 p1, float w1, u32x4& fp, u32x4& fm) {
  float mx = fmax_(fmax_(fmax_(p0[0], p0[1]), fmax_(p0[2], p0[3])), fmax_(fmax_(p1[0], p1[1]), fmax_(p1[2], p1[3])));
  float mn = fmin_(fmin_(fmin_(p0[0], p0[1]), fmin_(p0[2], p0[3])), fmin_(fmin_(p1[0], p1[1]), fmin_(p1[2], p1[3])));
  ROWRED(mx, fmax_); ROWRED(mn, fmin_);
  const float L = 1.4426950408889634f;
  const float a = -mx * L, b = mn * L;
  float ep[8], em[8];
#pragma unroll
  for (int i = 0; i < 4; ++i) {
    ep[i] = __builtin_amdgcn_exp2f(__builtin_fmaf(p0[i], L, a)); ep[4 + i] = __builtin_amdgcn_exp2f(__builtin_fmaf(p1[i], L, a));
    em[i] = __builtin_amdgcn_exp2f(__builtin_fmaf(p0[i], -L, b)); em[4 + i] = __builtin_amdgcn_exp2f(__builtin_fmaf(p1[i], -L, b));
  }
  float sp = __builtin_fmaf((ep[4] + ep[5]) + (ep[6] + ep[7]), w1, (ep[0] + ep[1]) + (ep[2] + ep[3]));
  float sm = __builtin_fmaf((em[4] + em[5]) + (em[6] + em[7]), w1, (em[0] + em[1]) + (em[2] + em[3]));
  ROWRED(sp, fadd_); ROWRED(sm, fadd_);
  const float ip = __builtin_amdgcn_rcpf(sp), im = __builtin_amdgcn_rcpf(sm), ip1 = ip * w1, im1 = im * w1;
  fp[0] = pk2(ep[0] * ip, ep[1] * ip); fp[1] = pk2(ep[2] * ip, ep[3] * ip); fp[2] = pk2(ep[4] * ip1, ep[5] * ip1); fp[3] = pk2(ep[6] * ip1, ep[7] * ip1);
  fm[0] = pk2(em[0] * im, em[1] * im); fm[1] = pk2(em[2] * im, em[3] * im); fm[2] = pk2(em[4] * im1, em[5] * im1); fm[3] = pk2(em[6] * im1, em[7] * im1);
}
DI u32x4 scale_pack(f32x4 a, f32x4 b, float s) {
  u32x4 r; r[0] = pk2(a[0] * s, a[1] * s); r[1] = pk2(a[2] * s, a[3] * s); r[2] = pk2(b[0] * s, b[1] * s); r[3] = pk2(b[2] * s, b[3] * s); return r;
}
DI void wait_flags(const char* ws, u32 off, int n, u64 epoch, int lane) {
  const GAS u64* p = (const GAS u64*)(ws + off) + (lane < n ? lane : n - 1);
#pragma unroll 1
  for (int spin = 0; spin < SPIN_CAP; ++spin) {
    const u64 v = __hip_atomic_load(p, __ATOMIC_RELAXED, __HIP_MEMORY_SCOPE_AGENT);
    if (__all(v == epoch)) break;
    __builtin_amdgcn_s_sleep(2);
  }
  __builtin_amdgcn_fence(__ATOMIC_ACQUIRE, "wavefront");
}
DI void st128(u32x4 v, srd_t s, u32 voff, u32 soff, int) { __builtin_amdgcn_raw_buffer_store_b128(v, s, voff + soff, 0, 16); }
DI void st128p(u32x4 v, srd_t s, u32 voff, u32 soff, int) { __builtin_amdgcn_raw_buffer_store_b128(v, s, voff + soff, 0, 0); }
DI void set_flag(char* ws, u32 off, u64 epoch) { __hip_atomic_store((GAS u64*)(ws + off), epoch, __ATOMIC_RELAXED, __HIP_MEMORY_SCOPE_AGENT); }

template <bool Q>
DI void body(const char* __restrict__ x, const char* __restrict__ wqkv, const char* __restrict__ wphi,
    const char* __restrict__ wo, const char* __restrict__ wg, const char* __restrict__ wu, const char* __restrict__ wd, char* __restrict__ out, char* ws) {
  extern __shared__ __attribute__((aligned(16))) char smem_[];
  LAS char* lds = (LAS char*)smem_;
  const u32 ldsb = (u32)(size_t)lds;
  const int tid = threadIdx.x, lane = tid & 63;
  const u32 w = rfl(tid >> 6), m = w & 3, n = w >> 2;
  const u32 c16 = lane & 15, g = lane >> 4, rho = lane >> 3, cc = lane & 7, q4 = c16 >> 2, p4 = lane & 3;
  const u32 b = blockIdx.x, xcd = b & 7, idx = b >> 3, pair = xcd >> 1, ch = xcd & 1;
  const u32 rg = 4 * pair + (idx >> 3), jj = Q ? 7 : (idx & 7);
  const u32 prod = 7 * ch + jj;
  u32 head = 8 * ch + jj;
  if (ch == 0) { if (jj == 6) head = 7; if (jj == 7) head = 6; } else { if (jj == 1) head = 15; if (jj == 7) head = 9; }
  const u32 rh = 8 * pair + 4 * ch + (idx >> 3);
  STAMP_DECL
  STAMP(0);
  const u64 epoch = *(const __attribute__((address_space(4))) u64*)(ws + WS_EPOCH) + 0x9E3779B97F4A7C15ull;

  const srd_t sX = mk_srd(x, S_ * XS), sQ = mk_srd(wqkv, D_ * QS), sP = mk_srd(wphi, 16 * 1568), sW = mk_srd(ws, WS_END);
  const srd_t sU0 = mk_srd(Q ? wqkv : wg, Q ? D_ * QS : D_ * GS), sU1 = mk_srd(Q ? wqkv : wu, Q ? D_ * QS : D_ * GS);
  const srd_t sD = mk_srd(wd, F_ * XS), sO = mk_srd(wo, D_ * XS);

  {
    const u32 vx0 = (16 * w + rho) * XS + 16 * (cc ^ (rho >> 1)), vx1 = (16 * w + 8 + rho) * XS + 16 * (cc ^ (4 + (rho >> 1)));
#pragma unroll
    for (int j = 0; j < 7; ++j) {
      dma16(ldsb + j * SLOT + w * 2048, vx0, sX, rg * 128 * XS + 128 * j);
      dma16(ldsb + j * SLOT + w * 2048 + 1024, vx1, sX, rg * 128 * XS + 128 * j);
    }
  }
  const u32 hwin = (56 * head) & ~15u;
  auto kvoff = [&](u32 row, u32 sg) __attribute__((always_inline)) { const u32 lc = cc ^ (sg << 1); return row * QS + 896 + hwin + (lc < 4 ? 16 * lc : 896 + 16 * (lc - 4)); };
  {
    const u32 vk0 = kvoff(16 * w + rho, (rho >> 1) & 1), vk1 = kvoff(16 * w + 8 + rho, ((rho >> 1) & 1) | 2);
#pragma unroll
    for (int i = 0; i < 3; ++i) {
      dma16(ldsb + (7 + i) * SLOT + w * 2048, vk0, sQ, 128 * i * QS);
      dma16(ldsb + (7 + i) * SLOT + w * 2048 + 1024, vk1, sQ, 128 * i * QS);
    }
  }
  asm volatile("" :: "s"(epoch));

  const u32 sgU = ((rho >> 1) & 1) | ((w & 1) << 1), lcU = cc ^ (sgU << 1);
  const u32 vU = Q ? (8 * w + rho) * QS + (lcU < 4 ? 16 * lcU : 16 * lcU - 16) : (8 * w + rho) * GS + 16 * lcU;
  const u32 strU = Q ? 64 * QS : 64 * GS;
  const u32 sofU0 = Q ? 56 * (8 * ch) : 256 * prod, sofU1 = Q ? 56 * (8 * ch + 2) : 256 * prod, sofUu = Q ? 224 : 128;
  const u32 vD = rho * XS + 16 * (cc ^ (((rho >> 1) & 3) << 1));
  auto issueU = [&](int u, int c, int slot) __attribute__((always_inline)) {
    dma16(ldsb + slot * SLOT + w * 1024, vU, sU0, sofU0 + u * sofUu + c * strU);
    dma16(ldsb + slot * SLOT + 8192 + w * 1024, vU, sU1, sofU1 + u * sofUu + c * strU);
  };
  auto issueD = [&](int d) __attribute__((always_inline)) {
    const int i = d >> 1, slot = (25 + d) % 10;
    const u32 kap = w + 8 * i;
    u32 row;
    if (i <= 6) { const u32 u = kap >= 28 ? 1 : 0, r = kap - 28 * u; row = 128 * (r >> 1) + 64 * u + 32 * (r & 1); } else row = (kap - 56) * 32;
    const u32 so = (row + 16 * (d & 1)) * XS + 128 * jj;
    dma16(ldsb + slot * SLOT + w * 2048, vD, i <= 6 ? sD : sO, so);
    dma16(ldsb + slot * SLOT + w * 2048 + 1024, vD, i <= 6 ? sD : sO, so + 8 * XS);
  };
  auto issue = [&](int t) __attribute__((always_inline)) {
    if (t == 10) {
      dma16(ldsb + w * 1024, kvoff(8 * w + rho, sgU), sQ, 384 * QS);
      dma16(ldsb + 8192 + (w & 1) * 1024, lane * 16, sP, head * 1568 + (w & 1) * 1024);
    } else if (t <= 17) issueU(0, t - 11, t % 10);
    else if (t <= 24) issueU(1, t - 18, t % 10);
  };

  u32x4 xa[14], xb[14];
  float s1 = 0.f, s2 = 0.f;
  {
    const u32 xr = (32 * m + 16 * n + c16) * 128 + 16 * (g ^ (c16 >> 1));
#pragma unroll
    for (int j = 0; j < 7; ++j) {
      if (j == 0) WAITV(18); else WAITV(16);
      BARRIER();
      if (j >= 1) issue(j + 9);
      LAS char* sl = lds + j * SLOT;
      xa[2 * j] = *(LAS u32x4*)(sl + xr); xa[2 * j + 1] = *(LAS u32x4*)(sl + (xr ^ 64));
      xb[2 * j] = *(LAS u32x4*)(sl + (xr ^ 2048)); xb[2 * j + 1] = *(LAS u32x4*)(sl + (xr ^ 2048 ^ 64));
#pragma unroll
      for (int i = 0; i < 4; ++i) {
        s1 = dot2(xa[2 * j][i], 0x3f803f80u, s1); s2 = dot2(xa[2 * j][i], xa[2 * j][i], s2);
        s1 = dot2(xa[2 * j + 1][i], 0x3f803f80u, s1); s2 = dot2(xa[2 * j + 1][i], xa[2 * j + 1][i], s2);
      }
    }
  }
  STAMP(1);
  ROWRED(s1, fadd_); ROWRED(s2, fadd_);
  float nma, rsa;
  {
    const float mean = s1 * (1.f / 448.f);
    const float var = fmax_(__builtin_fmaf(-mean, mean, s2 * (1.f / 448.f)), 0.f);
    nma = -rbf(mean);
    rsa = 1.f / rbf(__builtin_sqrtf(rbf(rbf(var) + 1e-5f)));
    f32x2 st = {nma, rsa};
    if (g == 0) *(LAS f32x2*)(lds + 10240 + (32 * m + 16 * n + c16) * 8) = st;
  }
  const u32 sig = (q4 >> 1) | ((g & 1) << 1);
  const float w1 = g == 3 ? 0.f : 1.f;
  float nmb, rsb;

  f32x4 ak[4] = {};
  {
    const u32 o = (head & 1) * 8;
    u32 lb0 = o + 8 * p4, lb1 = o + 32 + 8 * p4; if (lb1 >= 64) lb1 -= 8;
    const u32 rowb = (8 * g + q4) * 128;
    const u32 r0 = rowb + ((((lb0 >> 5) ^ sig) << 5) | (lb0 & 31)), r1 = rowb + ((((lb1 >> 5) ^ sig) << 5) | (lb1 & 31));
#pragma unroll
    for (int i = 0; i < 4; ++i) {
      if (i < 3) WAITV(16); else WAITV(14);
      BARRIER();
      if (i < 2) issue(16 + i);
      if (i == 0) { const f32x2 st = *(LAS f32x2*)(lds + 10240 + (32 * m + 16 * (n ^ 1) + c16) * 8); nmb = st[0]; rsb = st[1]; }
      LAS char* sl = lds + ((7 + i) % 10) * SLOT;
#pragma unroll
      for (int s = 0; s < (i < 3 ? 4 : 2); ++s) {
        LAS char* p = sl + s * 4096;
        const bf16x8 f0 = trfrag(p + r0, p + r0 + 512), f1 = trfrag(p + r1, p + r1 + 512);
        const bf16x8 f2 = trfrag(p + (r0 ^ 64), p + (r0 ^ 64) + 512), f3 = trfrag(p + (r1 ^ 64), p + (r1 ^ 64) + 512);
        xa[4 * i + s] = center(xa[4 * i + s], nma);
        const bf16x8 xf = as_frag(xa[4 * i + s]);
        ak[0] = mfma(f0, xf, ak[0]); ak[1] = mfma(f1, xf, ak[1]); ak[2] = mfma(f2, xf, ak[2]); ak[3] = mfma(f3, xf, ak[3]);
      }
    }
  }
  STAMP(2);
  const u32 wr0 = (4 * g + q4) * 56 + 8 * p4, wr1 = (4 * g + q4) * 56 + 32 + 8 * (p4 == 3 ? 2 : p4);
  auto wphi_frag = [&](LAS char* img, u32 a) __attribute__((always_inline)) {
    u32x4 f = __builtin_bit_cast(u32x4, trfrag(img + a, img + a + 16 * 56));
    if (g == 3) { f[2] = 0; f[3] = 0; }
    return as_frag(f);
  };
  {
    const bf16x8 wf0 = wphi_frag(lds + 8192, wr0), wf1 = wphi_frag(lds + 8192, wr1);
    const bf16x8 kf = as_frag(scale_pack(ak[0], ak[1], rsa));
    const f32x4 z = {0.f, 0.f, 0.f, 0.f};
    u32x4 fp, fm;
    dual_softmax(mfma(wf0, kf, z), mfma(wf1, kf, z), w1, fp, fm);
    u32x4 vf = scale_pack(ak[2], ak[3], rsa);
    if (g == 3) { vf[2] = 0x00003f80u; vf[3] = 0; }
    const u32 tok = 32 * m + 16 * n + c16, st = ((c16 >> 1) & 1) | (((c16 >> 3) & 1) << 1);
    LAS char* pk = lds + 8 * SLOT + tok * 128 + 8 * g;
    *(LAS u32x2*)(pk + ((0 ^ st) << 5)) = u32x2{fp[0], fp[1]}; *(LAS u32x2*)(pk + ((1 ^ st) << 5)) = u32x2{fp[2], fp[3]};
    *(LAS u32x2*)(pk + ((2 ^ st) << 5)) = u32x2{fm[0], fm[1]}; *(LAS u32x2*)(pk + ((3 ^ st) << 5)) = u32x2{fm[2], fm[3]};
    LAS char* pv = lds + 9 * SLOT + tok * 64 + 8 * g;
    *(LAS u32x2*)(pv + ((0 ^ (c16 >> 3)) << 5)) = u32x2{vf[0], vf[1]}; *(LAS u32x2*)(pv + ((1 ^ (c16 >> 3)) << 5)) = u32x2{vf[2], vf[3]};
    BARRIER();
    f32x4 kv = z;
    LAS char* ra = lds + 8 * SLOT + (8 * g + q4) * 128 + (((w >> 1) ^ sig) << 5) + 8 * p4;
    LAS char* rb = lds + 9 * SLOT + (8 * g + q4) * 64 + (((w & 1) ^ (g & 1)) << 5) + 8 * p4;
#pragma unroll
    for (int s = 0; s < 4; ++s) kv = mfma(trfrag(ra + s * 4096, ra + s * 4096 + 512), trfrag(rb + s * 2048, rb + s * 2048 + 256), kv);
    st128(__builtin_bit_cast(u32x4, kv), sW, lane * 16, WS_PART + (head * 16 + rg) * 8192 + w * 1024, 16);
  }
  STAMP(3);

  u32 ra[4];
  {
    const u32 rowb = (8 * g + q4) * 128;
    if (Q) {
      u32 lb[4] = {8 * p4, 32 + 8 * p4, 72 + 8 * p4, 104 + 8 * (p4 == 3 ? 2 : p4)};
#pragma unroll
      for (int i = 0; i < 4; ++i) ra[i] = n * 8192 + rowb + ((((lb[i] >> 5) ^ sig) << 5) | (lb[i] & 31));
    } else {
      ra[0] = rowb + (((2 * n) ^ sig) << 5) + 8 * p4; ra[1] = ra[0] ^ 32; ra[2] = ra[0] + 8192; ra[3] = ra[1] + 8192;
    }
  }
  u32x4 phq[2][2][2][2];
  const u32 ttile = rg * 8 + 2 * m;
#pragma unroll
  for (int u = 0; u < 2; ++u) {
    f32x4 acc[2][4] = {};
    bf16x8 F1[4];
#pragma unroll
    for (int c = 0; c < 7; ++c) {
      const int t = 11 + 7 * u + c;
      if (t == 11) WAITV(12);
      else if (t == 12) WAITV(16);
      else if (t == 13) WAITV(8);
      else if (t == 18) { if (Q) WAITV(0); else WAITV(16); }
      else if (t == 20) { if (Q) WAITV(0); else WAITV(4); }
      else if (t > 18) { if (!Q) WAITV(16); }
      BARRIER();
      if (t == 11) { issue(18); issue(19); }
      if (t + 9 <= 24) issue(t + 9);
      else if (!Q) issueD(t + 9 - 25);
      else if (t + 9 == 25) {
        dma16(ldsb + 5 * SLOT + w * 2048, lane * 16, sP, 8 * ch * 1568 + w * 2048);
        dma16(ldsb + 5 * SLOT + w * 2048 + 1024, lane * 16, sP, 8 * ch * 1568 + w * 2048 + 1024);
      }
      if (t == 13 && tid == 0) set_flag(ws, WS_FP + (head * 16 + rg) * 8, epoch);
      if (t == 20 && tid == 0) set_flag(ws, Q ? WS_FR + (rg * 2 + ch) * 8 : WS_FH + ((rg * 2) * 16 + prod) * 8, epoch);
      LAS char* sl = lds + (t % 10) * SLOT;
      bf16x8 F0[4];
#pragma unroll
      for (int i = 0; i < 4; ++i) F0[i] = trfrag(sl + ra[i], sl + ra[i] + 512);
      if (c > 0) {
        if (u == 0) xb[2 * c - 1] = center(xb[2 * c - 1], nmb);
#pragma unroll
        for (int i = 0; i < 4; ++i) { acc[0][i] = mfma(F1[i], as_frag(xa[2 * c - 1]), acc[0][i]); acc[1][i] = mfma(F1[i], as_frag(xb[2 * c - 1]), acc[1][i]); }
      }
#pragma unroll
      for (int i = 0; i < 4; ++i) F1[i] = trfrag(sl + ra[i] + 4096, sl + ra[i] + 4096 + 512);
      if (u == 0) xb[2 * c] = center(xb[2 * c], nmb);
#pragma unroll
      for (int i = 0; i < 4; ++i) { acc[0][i] = mfma(F0[i], as_frag(xa[2 * c]), acc[0][i]); acc[1][i] = mfma(F0[i], as_frag(xb[2 * c]), acc[1][i]); }
    }
    if (u == 0) xb[13] = center(xb[13], nmb);
#pragma unroll
    for (int i = 0; i < 4; ++i) { acc[0][i] = mfma(F1[i], as_frag(xa[13]), acc[0][i]); acc[1][i] = mfma(F1[i], as_frag(xb[13]), acc[1][i]); }
    STAMP(4 + 2 * u);
    if (!Q) {
#pragma unroll
      for (int tt = 0; tt < 2; ++tt) {
        const float rs = tt ? rsb : rsa, r2 = rs * rs;
        f32x4 h0, h1;
#pragma unroll
        for (int r = 0; r < 4; ++r) { h0[r] = fmax_(acc[tt][0][r], 0.f) * acc[tt][2][r]; h1[r] = fmax_(acc[tt][1][r], 0.f) * acc[tt][3][r]; }
        st128(scale_pack(h0, h1, r2), sW, lane * 16, WS_HB + ((ttile + (n ^ tt)) * 70 + 28 * u + 2 * prod + n) * 1024, 16);
      }
    } else {
      if (u == 0) { WAITV(0); BARRIER(); }
      u32x2 part[16];
      const u32 rhead = rg, rs_ = ch;
      if (u == 0) {
        wait_flags(ws, WS_FP + rhead * 128, 16, epoch, lane);
#pragma unroll
        for (int i = 0; i < 16; ++i)
          part[i] = __builtin_amdgcn_raw_buffer_load_b64(sW, lane * 8, WS_PART + (rhead * 16 + i) * 8192 + rs_ * 4096 + w * 512, 16);
      }
      const f32x4 z = {0.f, 0.f, 0.f, 0.f};
#pragma unroll
      for (int hh = 0; hh < 2; ++hh) {
        LAS char* img = lds + 5 * SLOT + (4 * u + 2 * n + hh) * 1568;
        const bf16x8 wf0 = wphi_frag(img, wr0), wf1 = wphi_frag(img, wr1);
#pragma unroll
        for (int tt = 0; tt < 2; ++tt) {
          const bf16x8 qf = as_frag(scale_pack(acc[tt][2 * hh], acc[tt][2 * hh + 1], tt ? rsb : rsa));
          dual_softmax(mfma(wf0, qf, z), mfma(wf1, qf, z), w1, phq[u][hh][tt][0], phq[u][hh][tt][1]);
          asm volatile("" : "+v"(phq[u][hh][tt][0]), "+v"(phq[u][hh][tt][1]));
        }
      }
      if (u == 0) {
        float a0 = 0.f, a1 = 0.f;
#pragma unroll
        for (int i = 0; i < 16; ++i) { a0 += __uint_as_float(part[i][0]); a1 += __uint_as_float(part[i][1]); }
        __builtin_amdgcn_raw_buffer_store_b32(pk2(a0, a1), sW, (lane >> 1) * 16 + (lane & 1) * 4,
            WS_KVF + rhead * 4096 + (rs_ * 2 + ((w >> 1) & 1)) * 1024 + (w & 1) * 512 + (w >> 2) * 8, 16);
      }
    }
    STAMP(5 + 2 * u);
  }

  if (Q) {
    wait_flags(ws, WS_FR + ch * 128, 16, epoch, lane);
    u32x4 kf[4];
#pragma unroll
    for (int i = 0; i < 4; ++i) kf[i] = __builtin_amdgcn_raw_buffer_load_b128(sW, lane * 16, WS_KVF + (8 * ch + w) * 4096 + i * 1024, 16);
    BARRIER();
#pragma unroll
    for (int i = 0; i < 4; ++i) *(LAS u32x4*)(lds + w * 4096 + i * 1024 + lane * 16) = kf[i];
    BARRIER();
    STAMP(8);
    const f32x4 z = {0.f, 0.f, 0.f, 0.f};
#pragma unroll
    for (int u = 0; u < 2; ++u)
#pragma unroll
      for (int hh = 0; hh < 2; ++hh) {
        const u32 hl = 4 * u + 2 * n + hh;
        LAS char* kp = lds + hl * 4096 + lane * 16;
        const bf16x8 k00 = *(LAS bf16x8*)(kp), k01 = *(LAS bf16x8*)(kp + 1024), k10 = *(LAS bf16x8*)(kp + 2048), k11 = *(LAS bf16x8*)(kp + 3072);
        const u32 c0 = 28 * hl + 4 * g, c1 = c0 + 16;
        const u32 o0 = (c0 >> 5) * 1024 + (((c0 >> 2) & 3) * 16 + c16) * 16 + ((c0 >> 4) & 1) * 8;
        const u32 o1 = (c1 >> 5) * 1024 + (((c1 >> 2) & 3) * 16 + c16) * 16 + ((c1 >> 4) & 1) * 8;
#pragma unroll
        for (int tt = 0; tt < 2; ++tt) {
          const f32x4 n0 = mfma(k10, as_frag(phq[u][hh][tt][1]), mfma(k00, as_frag(phq[u][hh][tt][0]), z));
          const f32x4 n1 = mfma(k11, as_frag(phq[u][hh][tt][1]), mfma(k01, as_frag(phq[u][hh][tt][0]), z));
          auto a_ = __builtin_amdgcn_permlane16_swap(__float_as_uint(n1[0]), __float_as_uint(n1[0]), false, false);
          auto b_ = __builtin_amdgcn_permlane32_swap(a_[1], a_[1], false, false);
          const float inv = __builtin_amdgcn_rcpf(__uint_as_float(b_[1]) + 1e-6f);
          const u32x4 of = scale_pack(n0, n1, inv);
          LAS char* ob = lds + 2 * SLOT + (2 * m + (n ^ tt)) * 7168;
          *(LAS u32x2*)(ob + o0) = u32x2{of[0], of[1]};
          if (g != 3) *(LAS u32x2*)(ob + o1) = u32x2{of[2], of[3]};
        }
      }
    BARRIER();
#pragma unroll
    for (int i = 0; i < 7; ++i) {
      const u32x4 v = *(LAS u32x4*)(lds + 2 * SLOT + w * 7168 + i * 1024 + lane * 16);
      st128(v, sW, lane * 16, WS_HB + ((rg * 8 + w) * 70 + 56 + 7 * ch + i) * 1024, 16);
    }
    WAITV(0);
    BARRIER();
    if (tid == 0) set_flag(ws, WS_FO + (rg * 16 + ch) * 8, epoch);
    STAMP(9);
    STAMP_FLUSH();
    asm volatile("kcode_end:");
    return;
  }

  WAITV(0);
  BARRIER();
  if (tid == 0) set_flag(ws, WS_FH + ((rg * 2 + 1) * 16 + prod) * 8, epoch);
  issueD(9);
  const u32 rg5 = rh >> 1;
  const u32x4 xres = __builtin_amdgcn_raw_buffer_load_b128(sX, (8 * w + rho) * XS + 16 * cc, rh * 64 * XS + 128 * jj, 0);
  f32x4 acc[4][4] = {};
  u32 rb[4];
#pragma unroll
  for (int i = 0; i < 4; ++i) rb[i] = w * 2048 + (4 * g + q4) * 128 + ((i ^ sig) << 5) + 8 * p4;
  u32x4 A[9][4];
  auto loadA = [&](int i) __attribute__((always_inline)) {
#pragma unroll
    for (int tt = 0; tt < 4; ++tt) A[i][tt] = __builtin_amdgcn_raw_buffer_load_b128(sW, lane * 16, WS_HB + ((rh * 4 + tt) * 70 + w + 8 * i) * 1024, 16);
  };
  auto kstep = [&](int i) __attribute__((always_inline)) {
    LAS char* p0 = lds + ((25 + 2 * i) % 10) * SLOT; LAS char* p1 = lds + ((26 + 2 * i) % 10) * SLOT;
    bf16x8 B[4];
#pragma unroll
    for (int ct = 0; ct < 4; ++ct) B[ct] = trfrag(p0 + rb[ct], p1 + rb[ct]);
    if (i + 5 <= 8) { asm volatile("s_waitcnt lgkmcnt(0)" : "+v"(B[0]), "+v"(B[1]), "+v"(B[2]), "+v"(B[3]) :: "memory"); issueD(2 * i + 10); issueD(2 * i + 11); }
#pragma unroll
    for (int tt = 0; tt < 4; ++tt)
#pragma unroll
      for (int ct = 0; ct < 4; ++ct) acc[tt][ct] = mfma(B[ct], as_frag(A[i][tt]), acc[tt][ct]);
  };
  wait_flags(ws, WS_FH + (rg5 * 2) * 128, 14, epoch, lane);
  STAMP(8);
  loadA(0); loadA(1); loadA(2);
  wait_flags(ws, WS_FH + (rg5 * 2 + 1) * 128, 14, epoch, lane);
  STAMP(9);
  loadA(3); loadA(4); loadA(5); loadA(6);
  kstep(0); kstep(1); kstep(2);
  wait_flags(ws, WS_FO + rg5 * 128, 2, epoch, lane);
  STAMP(10);
  loadA(7); if (w < 6) loadA(8);
  kstep(3); kstep(4); kstep(5); kstep(6);
  WAITV(0);
  kstep(7); if (w < 6) kstep(8);
  STAMP(11);
  asm volatile("s_waitcnt lgkmcnt(0)" ::: "memory");
#pragma unroll
  for (int tt = 0; tt < 4; ++tt)
#pragma unroll
    for (int ct = 0; ct < 4; ++ct)
      *(LAS f32x4*)(lds + (2 * tt + (c16 >> 3)) * SLOT + w * 2048 + (c16 & 7) * 256 + (((4 * ct + g) ^ (c16 & 7)) << 4)) = acc[tt][ct];
  BARRIER();
  f32x4 r0, r1;
#pragma unroll
  for (int i = 0; i < 4; ++i) {
    const u32 xv = xres[i];
    const float lo = __uint_as_float(xv << 16), hi = __uint_as_float(xv & 0xffff0000u);
    if (i < 2) { r0[2 * i] = lo; r0[2 * i + 1] = hi; } else { r1[2 * i - 4] = lo; r1[2 * i - 3] = hi; }
  }
#pragma unroll
  for (int j = 0; j < 8; ++j) {
    LAS char* p = lds + w * SLOT + j * 2048 + rho * 256;
    r0 += *(LAS f32x4*)(p + (((2 * cc) ^ rho) << 4)); r1 += *(LAS f32x4*)(p + (((2 * cc + 1) ^ rho) << 4));
  }
  const srd_t sOut = mk_srd(out, S_ * XS);
  st128p(scale_pack(r0, r1, 1.f), sOut, (8 * w + rho) * XS + 16 * cc, rh * 64 * XS + 128 * jj, 0);
  if (b == 0 && tid == 0) *(u64*)(ws + WS_EPOCH) = epoch;
  STAMP(12);
  STAMP_FLUSH();
}

__global__ __launch_bounds__(512, 2) void fused_kernel(const char* __restrict__ x, const char* __restrict__ wqkv, const char* __restrict__ wphi,
    const char* __restrict__ wo, const char* __restrict__ wg, const char* __restrict__ wu, const char* __restrict__ wd, char* __restrict__ out, char* ws) {
  {
    u64 pc; u32 len;
    asm volatile("kcode_begin:\n\ts_getpc_b64 %0\n\ts_mov_b32 %1, kcode_end-kcode_begin" : "=s"(pc), "=s"(len));
    const srd_t sc = mk_srd((const void*)(pc & ~(u64)127), len);
    asm volatile("s_mov_b32 m0, %0\n\ts_nop 0\n\tbuffer_load_dword %1, %2, 0 offen lds" :: "s"(9 * SLOT + rfl(threadIdx.x >> 6) * 2048), "v"(((blockIdx.x >> 3) * 512 + threadIdx.x) * 4), "s"(sc) : "memory");
  }
  if (((blockIdx.x >> 3) & 7) == 7) body<true>(x, wqkv, wphi, wo, wg, wu, wd, out, ws);
  else body<false>(x, wqkv, wphi, wo, wg, wu, wd, out, ws);
}

extern "C" void kernel_launch(void* const* d_in, const int* in_sizes, int n_in, void* d_out, int out_size, void* d_ws, size_t ws_size, hipStream_t stream) {
  (void)in_sizes; (void)n_in; (void)out_size; (void)ws_size;
  (void)hipFuncSetAttribute((const void*)fused_kernel, hipFuncAttributeMaxDynamicSharedMemorySize, 163840);
  fused_kernel<<<256, 512, 163840, stream>>>((const char*)d_in[0], (const char*)d_in[1], (const char*)d_in[2], (const char*)d_in[3],
      (const char*)d_in[5], (const char*)d_in[6], (const char*)d_in[7], (char*)d_out, (char*)d_ws);
}
```

```cpp
#include <hip/hip_runtime.h>
#include <stdint.h>

typedef unsigned u32;
typedef unsigned long long u64;
typedef short bf16x8 __attribute__((ext_vector_type(8)));
typedef short s16x4 __attribute__((ext_vector_type(4)));
typedef float f32x4 __attribute__((ext_vector_type(4)));
typedef float f32x2 __attribute__((ext_vector_type(2)));
typedef u32 u32x4 __attribute__((ext_vector_type(4)));
typedef u32 u32x2 __attribute__((ext_vector_type(2)));
typedef __bf16 bf16x2 __attribute__((ext_vector_type(2)));
typedef __amdgpu_buffer_rsrc_t srd_t;
#define LAS __attribute__((address_space(3)))
#define GAS __attribute__((address_space(1)))
#define DI __device__ __forceinline__

constexpr int S_ = 2048, D_ = 448, F_ = 1792;
constexpr u32 XS = 896, QS = 2688, GS = 3584;
constexpr u32 SLOT = 16384;
constexpr u32 WS_EPOCH = 0, WS_FP = 1024, WS_FR = 3072, WS_FH = 4096, WS_FO = 8192, WS_KVF = 16384,
              WS_PART = 131072, WS_HB = 4194304, WS_END = WS_HB + 128u * 70u * 1024u, WS_DBG = 16u << 20;
constexpr int SPIN_CAP = 1 << 15;

#define STAMP(i) do {} while (0)
#define STAMP_DECL
#define STAMP_FLUSH() do {} while (0)

DI u32 rfl(u32 x) { return __builtin_amdgcn_readfirstlane(x); }
DI srd_t mk_srd(const void* p, u32 bytes) { return __builtin_amdgcn_make_buffer_rsrc((void*)p, (short)0, (int)bytes, 0x00020000); }
DI void dma16(u32 m0v, u32 voff, srd_t srd, u32 soff) {
  asm volatile("s_mov_b32 m0, %0\n\ts_nop 0\n\tbuffer_load_dwordx4 %1, %2, %3 offen lds" :: "s"(m0v), "v"(voff), "s"(srd), "s"(soff) : "memory");
}
#define WAITV(n) asm volatile("s_waitcnt vmcnt(" #n ")" ::: "memory")
#define BARRIER() asm volatile("s_waitcnt lgkmcnt(0)\n\ts_barrier" ::: "memory")
DI f32x4 mfma(bf16x8 a, bf16x8 b, f32x4 c) { return __builtin_amdgcn_mfma_f32_16x16x32_bf16(a, b, c, 0, 0, 0); }
DI bf16x8 trfrag(LAS char* p0, LAS char* p1) {
  s16x4 lo = __builtin_amdgcn_ds_read_tr16_b64_v4i16((LAS s16x4*)p0);
  s16x4 hi = __builtin_amdgcn_ds_read_tr16_b64_v4i16((LAS s16x4*)p1);
  return __builtin_shufflevector(lo, hi, 0, 1, 2, 3, 4, 5, 6, 7);
}
DI u32 pk2(float a, float b) { f32x2 f = {a, b}; return __builtin_bit_cast(u32, __builtin_convertvector(f, bf16x2)); }
DI float rbf(float a) { return __uint_as_float(pk2(a, a) << 16); }
DI float dot2(u32 a, u32 b, float c) { return __builtin_amdgcn_fdot2_f32_bf16(__builtin_bit_cast(bf16x2, a), __builtin_bit_cast(bf16x2, b), c, false); }
DI bf16x8 as_frag(u32x4 v) { return __builtin_bit_cast(bf16x8, v); }
#define ROWRED(x, OP) do { auto a_ = __builtin_amdgcn_permlane16_swap(__float_as_uint(x), __float_as_uint(x), false, false); \
    x = OP(__uint_as_float(a_[0]), __uint_as_float(a_[1])); \
    auto b_ = __builtin_amdgcn_permlane32_swap(__float_as_uint(x), __float_as_uint(x), false, false); \
    x = OP(__uint_as_float(b_[0]), __uint_as_float(b_[1])); } while (0)
DI float fadd_(float a, float b) { return a + b; }
DI float fmax_(float a, float b) { return __builtin_fmaxf(a, b); }
DI float fmin_(float a, float b) { return __builtin_fminf(a, b); }

DI u32x4 center(u32x4 x, float nmb) {
  u32x4 r; u32 c0 = 0x00003f80u, c1 = 0x3f800000u;
  asm("" : "+s"(c0), "+s"(c1));
#pragma unroll
  for (int i = 0; i < 4; ++i) r[i] = pk2(dot2(x[i], c0, nmb), dot2(x[i], c1, nmb));
  return r;
}

DI void dual_softmax(f32x4 p0, f32x4 p1, float w1, u32x4& fp, u32x4& fm) {
  float mx = fmax_(fmax_(fmax_(p0[0], p0[1]), fmax_(p0[2], p0[3])), fmax_(fmax_(p1[0], p1[1]), fmax_(p1[2], p1[3])));
  float mn = fmin_(fmin_(fmin_(p0[0], p0[1]), fmin_(p0[2], p0[3])), fmin_(fmin_(p1[0], p1[1]), fmin_(p1[2], p1[3])));
  ROWRED(mx, fmax_); ROWRED(mn, fmin_);
  const float L = 1.4426950408889634f;
  const float a = -mx * L, b = mn * L;
  float ep[8], em[8];
#pragma unroll
  for (int i = 0; i < 4; ++i) {
    ep[i] = __builtin_amdgcn_exp2f(__builtin_fmaf(p0[i], L, a)); ep[4 + i] = __builtin_amdgcn_exp2f(__builtin_fmaf(p1[i], L, a));
    em[i] = __builtin_amdgcn_exp2f(__builtin_fmaf(p0[i], -L, b)); em[4 + i] = __builtin_amdgcn_exp2f(__builtin_fmaf(p1[i], -L, b));
  }
  float sp = __builtin_fmaf((ep[4] + ep[5]) + (ep[6] + ep[7]), w1, (ep[0] + ep[1]) + (ep[2] + ep[3]));
  float sm = __builtin_fmaf((em[4] + em[5]) + (em[6] + em[7]), w1, (em[0] + em[1]) + (em[2] + em[3]));
  ROWRED(sp, fadd_); ROWRED(sm, fadd_);
  const float ip = __builtin_amdgcn_rcpf(sp), im = __builtin_amdgcn_rcpf(sm), ip1 = ip * w1, im1 = im * w1;
  fp[0] = pk2(ep[0] * ip, ep[1] * ip); fp[1] = pk2(ep[2] * ip, ep[3] * ip); fp[2] = pk2(ep[4] * ip1, ep[5] * ip1); fp[3] = pk2(ep[6] * ip1, ep[7] * ip1);
  fm[0] = pk2(em[0] * im, em[1] * im); fm[1] = pk2(em[2] * im, em[3] * im); fm[2] = pk2(em[4] * im1, em[5] * im1); fm[3] = pk2(em[6] * im1, em[7] * im1);
}
DI u32x4 scale_pack(f32x4 a, f32x4 b, float s) {
  u32x4 r; r[0] = pk2(a[0] * s, a[1] * s); r[1] = pk2(a[2] * s, a[3] * s); r[2] = pk2(b[0] * s, b[1] * s); r[3] = pk2(b[2] * s, b[3] * s); return r;
}
DI void wait_flags(const char* ws, u32 off, int n, u64 epoch, int lane) {
  const GAS u64* p = (const GAS u64*)(ws + off) + (lane < n ? lane : n - 1);
#pragma unroll 1
  for (int spin = 0; spin < SPIN_CAP; ++spin) {
    const u64 v = __hip_atomic_load(p, __ATOMIC_RELAXED, __HIP_MEMORY_SCOPE_AGENT);
    if (__all(v == epoch)) break;
    __builtin_amdgcn_s_sleep(2);
  }
  __builtin_amdgcn_fence(__ATOMIC_ACQUIRE, "wavefront");
}
DI void st128(u32x4 v, srd_t s, u32 voff, u32 soff, int) { __builtin_amdgcn_raw_buffer_store_b128(v, s, voff + soff, 0, 16); }
DI void st128p(u32x4 v, srd_t s, u32 voff, u32 soff, int) { __builtin_amdgcn_raw_buffer_store_b128(v, s, voff + soff, 0, 0); }
DI void set_flag(char* ws, u32 off, u64 epoch) { __hip_atomic_store((GAS u64*)(ws + off), epoch, __ATOMIC_RELAXED, __HIP_MEMORY_SCOPE_AGENT); }

template <bool Q>
DI void body(const char* __restrict__ x, const char* __restrict__ wqkv, const char* __restrict__ wphi,
    const char* __restrict__ wo, const char* __restrict__ wg, const char* __restrict__ wu, const char* __restrict__ wd, char* __restrict__ out, char* ws) {
  extern __shared__ __attribute__((aligned(16))) char smem_[];
  LAS char* lds = (LAS char*)smem_;
  const u32 ldsb = (u32)(size_t)lds;
  const int tid = threadIdx.x, lane = tid & 63;
  const u32 w = rfl(tid >> 6), m = w & 3, n = w >> 2;
  const u32 c16 = lane & 15, g = lane >> 4, rho = lane >> 3, cc = lane & 7, q4 = c16 >> 2, p4 = lane & 3;
  const u32 b = blockIdx.x, xcd = b & 7, idx = b >> 3, pair = xcd >> 1, ch = xcd & 1;
  const u32 rg = 4 * pair + (idx >> 3), jj = Q ? 7 : (idx & 7);
  const u32 prod = 7 * ch + jj;
  u32 head = 8 * ch + jj;
  if (ch == 0) { if (jj == 6) head = 7; if (jj == 7) head = 6; } else { if (jj == 1) head = 15; if (jj == 7) head = 9; }
  const u32 rh = 8 * pair + 4 * ch + (idx >> 3);
  STAMP_DECL
  STAMP(0);
  const u64 epoch = *(const __attribute__((address_space(4))) u64*)(ws + WS_EPOCH) + 0x9E3779B97F4A7C15ull;

  const srd_t sX = mk_srd(x, S_ * XS), sQ = mk_srd(wqkv, D_ * QS), sP = mk_srd(wphi, 16 * 1568), sW = mk_srd(ws, WS_END);
  const srd_t sU0 = mk_srd(Q ? wqkv : wg, Q ? D_ * QS : D_ * GS), sU1 = mk_srd(Q ? wqkv : wu, Q ? D_ * QS : D_ * GS);
  const srd_t sD = mk_srd(wd, F_ * XS), sO = mk_srd(wo, D_ * XS);

  {
    const u32 vx0 = (16 * w + rho) * XS + 16 * (cc ^ (rho >> 1)), vx1 = (16 * w + 8 + rho) * XS + 16 * (cc ^ (4 + (rho >> 1)));
#pragma unroll
    for (int j = 0; j < 7; ++j) {
      dma16(ldsb + j * SLOT + w * 2048, vx0, sX, rg * 128 * XS + 128 * j);
      dma16(ldsb + j * SLOT + w * 2048 + 1024, vx1, sX, rg * 128 * XS + 128 * j);
    }
  }
  const u32 hwin = (56 * head) & ~15u;
  auto kvoff = [&](u32 row, u32 sg) __attribute__((always_inline)) { const u32 lc = cc ^ (sg << 1); return row * QS + 896 + hwin + (lc < 4 ? 16 * lc : 896 + 16 * (lc - 4)); };
  {
    const u32 vk0 = kvoff(16 * w + rho, (rho >> 1) & 1), vk1 = kvoff(16 * w + 8 + rho, ((rho >> 1) & 1) | 2);
#pragma unroll
    for (int i = 0; i < 3; ++i) {
      dma16(ldsb + (7 + i) * SLOT + w * 2048, vk0, sQ, 128 * i * QS);
      dma16(ldsb + (7 + i) * SLOT + w * 2048 + 1024, vk1, sQ, 128 * i * QS);
    }
  }
  asm volatile("" :: "s"(epoch));

  const u32 sgU = ((rho >> 1) & 1) | ((w & 1) << 1), lcU = cc ^ (sgU << 1);
  const u32 vU = Q ? (8 * w + rho) * QS + (lcU < 4 ? 16 * lcU : 16 * lcU - 16) : (8 * w + rho) * GS + 16 * lcU;
  const u32 strU = Q ? 64 * QS : 64 * GS;
  const u32 sofU0 = Q ? 56 * (8 * ch) : 256 * prod, sofU1 = Q ? 56 * (8 * ch + 2) : 256 * prod, sofUu = Q ? 224 : 128;
  const u32 vD = rho * XS + 16 * (cc ^ (((rho >> 1) & 3) << 1));
  auto issueU = [&](int u, int c, int slot) __attribute__((always_inline)) {
    dma16(ldsb + slot * SLOT + w * 1024, vU, sU0, sofU0 + u * sofUu + c * strU);
    dma16(ldsb + slot * SLOT + 8192 + w * 1024, vU, sU1, sofU1 + u * sofUu + c * strU);
  };
  auto issueD = [&](int d) __attribute__((always_inline)) {
    const int i = d >> 1, slot = (25 + d) % 10;
    const u32 kap = w + 8 * i;
    u32 row;
    if (i <= 6) { const u32 u = kap >= 28 ? 1 : 0, r = kap - 28 * u; row = 128 * (r >> 1) + 64 * u + 32 * (r & 1); } else row = (kap - 56) * 32;
    const u32 so = (row + 16 * (d & 1)) * XS + 128 * jj;
    dma16(ldsb + slot * SLOT + w * 2048, vD, i <= 6 ? sD : sO, so);
    dma16(ldsb + slot * SLOT + w * 2048 + 1024, vD, i <= 6 ? sD : sO, so + 8 * XS);
  };
  auto issue = [&](int t) __attribute__((always_inline)) {
    if (t == 10) {
      dma16(ldsb + w * 1024, kvoff(8 * w + rho, sgU), sQ, 384 * QS);
      dma16(ldsb + 8192 + (w & 1) * 1024, lane * 16, sP, head * 1568 + (w & 1) * 1024);
    } else if (t <= 17) issueU(0, t - 11, t % 10);
    else if (t <= 24) issueU(1, t - 18, t % 10);
  };

  u32x4 xa[14], xb[14];
  float s1 = 0.f, s2 = 0.f;
  {
    const u32 xr = (32 * m + 16 * n + c16) * 128 + 16 * (g ^ (c16 >> 1));
#pragma unroll
    for (int j = 0; j < 7; ++j) {
      if (j == 0) WAITV(18); else WAITV(16);
      BARRIER();
      if (j >= 1) issue(j + 9);
      LAS char* sl = lds + j * SLOT;
      xa[2 * j] = *(LAS u32x4*)(sl + xr); xa[2 * j + 1] = *(LAS u32x4*)(sl + (xr ^ 64));
      xb[2 * j] = *(LAS u32x4*)(sl + (xr ^ 2048)); xb[2 * j + 1] = *(LAS u32x4*)(sl + (xr ^ 2048 ^ 64));
#pragma unroll
      for (int i = 0; i < 4; ++i) {
        s1 = dot2(xa[2 * j][i], 0x3f803f80u, s1); s2 = dot2(xa[2 * j][i], xa[2 * j][i], s2);
        s1 = dot2(xa[2 * j + 1][i], 0x3f803f80u, s1); s2 = dot2(xa[2 * j + 1][i], xa[2 * j + 1][i], s2);
      }
    }
  }
  STAMP(1);
  ROWRED(s1, fadd_); ROWRED(s2, fadd_);
  float nma, rsa;
  {
    const float mean = s1 * (1.f / 448.f);
    const float var = fmax_(__builtin_fmaf(-mean, mean, s2 * (1.f / 448.f)), 0.f);
    nma = -rbf(mean);
    rsa = 1.f / rbf(__builtin_sqrtf(rbf(rbf(var) + 1e-5f)));
    f32x2 st = {nma, rsa};
    if (g == 0) *(LAS f32x2*)(lds + 10240 + (32 * m + 16 * n + c16) * 8) = st;
  }
  const u32 sig = (q4 >> 1) | ((g & 1) << 1);
  const float w1 = g == 3 ? 0.f : 1.f;
  float nmb, rsb;

  f32x4 ak[4] = {};
  {
    const u32 o = (head & 1) * 8;
    u32 lb0 = o + 8 * p4, lb1 = o + 32 + 8 * p4; if (lb1 >= 64) lb1 -= 8;
    const u32 rowb = (8 * g + q4) * 128;
    const u32 r0 = rowb + ((((lb0 >> 5) ^ sig) << 5) | (lb0 & 31)), r1 = rowb + ((((lb1 >> 5) ^ sig) << 5) | (lb1 & 31));
#pragma unroll
    for (int i = 0; i < 4; ++i) {
      if (i < 3) WAITV(16); else WAITV(14);
      BARRIER();
      if (i < 2) issue(16 + i);
      if (i == 0) { const f32x2 st = *(LAS f32x2*)(lds + 10240 + (32 * m + 16 * (n ^ 1) + c16) * 8); nmb = st[0]; rsb = st[1]; }
      LAS char* sl = lds + ((7 + i) % 10) * SLOT;
#pragma unroll
      for (int s = 0; s < (i < 3 ? 4 : 2); ++s) {
        LAS char* p = sl + s * 4096;
        const bf16x8 f0 = trfrag(p + r0, p + r0 + 512), f1 = trfrag(p + r1, p + r1 + 512);
        const bf16x8 f2 = trfrag(p + (r0 ^ 64), p + (r0 ^ 64) + 512), f3 = trfrag(p + (r1 ^ 64), p + (r1 ^ 64) + 512);
        xa[4 * i + s] = center(xa[4 * i + s], nma);
        const bf16x8 xf = as_frag(xa[4 * i + s]);
        ak[0] = mfma(f0, xf, ak[0]); ak[1] = mfma(f1, xf, ak[1]); ak[2] = mfma(f2, xf, ak[2]); ak[3] = mfma(f3, xf, ak[3]);
      }
    }
  }
  STAMP(2);
  const u32 wr0 = (4 * g + q4) * 56 + 8 * p4, wr1 = (4 * g + q4) * 56 + 32 + 8 * (p4 == 3 ? 2 : p4);
  auto wphi_frag = [&](LAS char* img, u32 a) __attribute__((always_inline)) {
    u32x4 f = __builtin_bit_cast(u32x4, trfrag(img + a, img + a + 16 * 56));
    if (g == 3) { f[2] = 0; f[3] = 0; }
    return as_frag(f);
  };
  {
    const bf16x8 wf0 = wphi_frag(lds + 8192, wr0), wf1 = wphi_frag(lds + 8192, wr1);
    const bf16x8 kf = as_frag(scale_pack(ak[0], ak[1], rsa));
    const f32x4 z = {0.f, 0.f, 0.f, 0.f};
    u32x4 fp, fm;
    dual_softmax(mfma(wf0, kf, z), mfma(wf1, kf, z), w1, fp, fm);
    u32x4 vf = scale_pack(ak[2], ak[3], rsa);
    if (g == 3) { vf[2] = 0x00003f80u; vf[3] = 0; }
    const u32 tok = 32 * m + 16 * n + c16, st = ((c16 >> 1) & 1) | (((c16 >> 3) & 1) << 1);
    LAS char* pk = lds + 8 * SLOT + tok * 128 + 8 * g;
    *(LAS u32x2*)(pk + ((0 ^ st) << 5)) = u32x2{fp[0], fp[1]}; *(LAS u32x2*)(pk + ((1 ^ st) << 5)) = u32x2{fp[2], fp[3]};
    *(LAS u32x2*)(pk + ((2 ^ st) << 5)) = u32x2{fm[0], fm[1]}; *(LAS u32x2*)(pk + ((3 ^ st) << 5)) = u32x2{fm[2], fm[3]};
    LAS char* pv = lds + 9 * SLOT + tok * 64 + 8 * g;
    *(LAS u32x2*)(pv + ((0 ^ (c16 >> 3)) << 5)) = u32x2{vf[0], vf[1]}; *(LAS u32x2*)(pv + ((1 ^ (c16 >> 3)) << 5)) = u32x2{vf[2], vf[3]};
    BARRIER();
    f32x4 kv = z;
    LAS char* ra = lds + 8 * SLOT + (8 * g + q4) * 128 + (((w >> 1) ^ sig) << 5) + 8 * p4;
    LAS char* rb = lds + 9 * SLOT + (8 * g + q4) * 64 + (((w & 1) ^ (g & 1)) << 5) + 8 * p4;
#pragma unroll
    for (int s = 0; s < 4; ++s) kv = mfma(trfrag(ra + s * 4096, ra + s * 4096 + 512), trfrag(rb + s * 2048, rb + s * 2048 + 256), kv);
    st128(__builtin_bit_cast(u32x4, kv), sW, lane * 16, WS_PART + (head * 16 + rg) * 8192 + w * 1024, 16);
  }
  STAMP(3);

  u32 ra[4];
  {
    const u32 rowb = (8 * g + q4) * 128;
    if (Q) {
      u32 lb[4] = {8 * p4, 32 + 8 * p4, 72 + 8 * p4, 104 + 8 * (p4 == 3 ? 2 : p4)};
#pragma unroll
      for (int i = 0; i < 4; ++i) ra[i] = n * 8192 + rowb + ((((lb[i] >> 5) ^ sig) << 5) | (lb[i] & 31));
    } else {
      ra[0] = rowb + (((2 * n) ^ sig) << 5) + 8 * p4; ra[1] = ra[0] ^ 32; ra[2] = ra[0] + 8192; ra[3] = ra[1] + 8192;
    }
  }
  u32x4 phq[2][2][2][2];
  const u32 ttile = rg * 8 + 2 * m;
#pragma unroll
  for (int u = 0; u < 2; ++u) {
    f32x4 acc[2][4] = {};
    bf16x8 F1[4];
#pragma unroll
    for (int c = 0; c < 7; ++c) {
      const int t = 11 + 7 * u + c;
      if (t == 11) WAITV(12);
      else if (t == 12) WAITV(16);
      else if (t == 13) WAITV(8);
      else if (t == 18) { if (Q) WAITV(0); else WAITV(16); }
      else if (t == 20) { if (Q) WAITV(0); else WAITV(4); }
      else if (t > 18) { if (!Q) WAITV(16); }
      BARRIER();
      if (t == 11) { issue(18); issue(19); }
      if (t + 9 <= 24) issue(t + 9);
      else if (!Q) issueD(t + 9 - 25);
      else if (t + 9 == 25) {
        dma16(ldsb + 5 * SLOT + w * 2048, lane * 16, sP, 8 * ch * 1568 + w * 2048);
        dma16(ldsb + 5 * SLOT + w * 2048 + 1024, lane * 16, sP, 8 * ch * 1568 + w * 2048 + 1024);
      }
      if (t == 13 && tid == 0) set_flag(ws, WS_FP + (head * 16 + rg) * 8, epoch);
      if (t == 20 && tid == 0) set_flag(ws, Q ? WS_FR + (rg * 2 + ch) * 8 : WS_FH + ((rg * 2) * 16 + prod) * 8, epoch);
      LAS char* sl = lds + (t % 10) * SLOT;
      bf16x8 F0[4];
#pragma unroll
      for (int i = 0; i < 4; ++i) F0[i] = trfrag(sl + ra[i], sl + ra[i] + 512);
      if (c > 0) {
        if (u == 0) xb[2 * c - 1] = center(xb[2 * c - 1], nmb);
#pragma unroll
        for (int i = 0; i < 4; ++i) { acc[0][i] = mfma(F1[i], as_frag(xa[2 * c - 1]), acc[0][i]); acc[1][i] = mfma(F1[i], as_frag(xb[2 * c - 1]), acc[1][i]); }
      }
#pragma unroll
      for (int i = 0; i < 4; ++i) F1[i] = trfrag(sl + ra[i] + 4096, sl + ra[i] + 4096 + 512);
      if (u == 0) xb[2 * c] = center(xb[2 * c], nmb);
#pragma unroll
      for (int i = 0; i < 4; ++i) { acc[0][i] = mfma(F0[i], as_frag(xa[2 * c]), acc[0][i]); acc[1][i] = mfma(F0[i], as_frag(xb[2 * c]), acc[1][i]); }
    }
    if (u == 0) xb[13] = center(xb[13], nmb);
#pragma unroll
    for (int i = 0; i < 4; ++i) { acc[0][i] = mfma(F1[i], as_frag(xa[13]), acc[0][i]); acc[1][i] = mfma(F1[i], as_frag(xb[13]), acc[1][i]); }
    STAMP(4 + 2 * u);
    if (!Q) {
#pragma unroll
      for (int tt = 0; tt < 2; ++tt) {
        const float rs = tt ? rsb : rsa, r2 = rs * rs;
        f32x4 h0, h1;
#pragma unroll
        for (int r = 0; r < 4; ++r) { h0[r] = fmax_(acc[tt][0][r], 0.f) * acc[tt][2][r]; h1[r] = fmax_(acc[tt][1][r], 0.f) * acc[tt][3][r]; }
        st128(scale_pack(h0, h1, r2), sW, lane * 16, WS_HB + ((ttile + (n ^ tt)) * 70 + 28 * u + 2 * prod + n) * 1024, 16);
      }
    } else {
      if (u == 0) { WAITV(0); BARRIER(); }
      u32x2 part[16];
      const u32 rhead = rg, rs_ = ch;
      if (u == 0) {
        wait_flags(ws, WS_FP + rhead * 128, 16, epoch, lane);
#pragma unroll
        for (int i = 0; i < 16; ++i)
          part[i] = __builtin_amdgcn_raw_buffer_load_b64(sW, lane * 8, WS_PART + (rhead * 16 + i) * 8192 + rs_ * 4096 + w * 512, 16);
      }
      const f32x4 z = {0.f, 0.f, 0.f, 0.f};
#pragma unroll
      for (int hh = 0; hh < 2; ++hh) {
        LAS char* img = lds + 5 * SLOT + (4 * u + 2 * n + hh) * 1568;
        const bf16x8 wf0 = wphi_frag(img, wr0), wf1 = wphi_frag(img, wr1);
#pragma unroll
        for (int tt = 0; tt < 2; ++tt) {
          const bf16x8 qf = as_frag(scale_pack(acc[tt][2 * hh], acc[tt][2 * hh + 1], tt ? rsb : rsa));
          dual_softmax(mfma(wf0, qf, z), mfma(wf1, qf, z), w1, phq[u][hh][tt][0], phq[u][hh][tt][1]);
          asm volatile("" : "+v"(phq[u][hh][tt][0]), "+v"(phq[u][hh][tt][1]));
        }
      }
      if (u == 0) {
        float a0 = 0.f, a1 = 0.f;
#pragma unroll
        for (int i = 0; i < 16; ++i) { a0 += __uint_as_float(part[i][0]); a1 += __uint_as_float(part[i][1]); }
        __builtin_amdgcn_raw_buffer_store_b32(pk2(a0, a1), sW, (lane >> 1) * 16 + (lane & 1) * 4,
            WS_KVF + rhead * 4096 + (rs_ * 2 + ((w >> 1) & 1)) * 1024 + (w & 1) * 512 + (w >> 2) * 8, 16);
      }
    }
    STAMP(5 + 2 * u);
  }

  if (Q) {
    wait_flags(ws, WS_FR + ch * 128, 16, epoch, lane);
    u32x4 kf[4];
#pragma unroll
    for (int i = 0; i < 4; ++i) kf[i] = __builtin_amdgcn_raw_buffer_load_b128(sW, lane * 16, WS_KVF + (8 * ch + w) * 4096 + i * 1024, 16);
    BARRIER();
#pragma unroll
    for (int i = 0; i < 4; ++i) *(LAS u32x4*)(lds + w * 4096 + i * 1024 + lane * 16) = kf[i];
    BARRIER();
    STAMP(8);
    const f32x4 z = {0.f, 0.f, 0.f, 0.f};
#pragma unroll
    for (int u = 0; u < 2; ++u)
#pragma unroll
      for (int hh = 0; hh < 2; ++hh) {
        const u32 hl = 4 * u + 2 * n + hh;
        LAS char* kp = lds + hl * 4096 + lane * 16;
        const bf16x8 k00 = *(LAS bf16x8*)(kp), k01 = *(LAS bf16x8*)(kp + 1024), k10 = *(LAS bf16x8*)(kp + 2048), k11 = *(LAS bf16x8*)(kp + 3072);
        const u32 c0 = 28 * hl + 4 * g, c1 = c0 + 16;
        const u32 o0 = (c0 >> 5) * 1024 + (((c0 >> 2) & 3) * 16 + c16) * 16 + ((c0 >> 4) & 1) * 8;
        const u32 o1 = (c1 >> 5) * 1024 + (((c1 >> 2) & 3) * 16 + c16) * 16 + ((c1 >> 4) & 1) * 8;
#pragma unroll
        for (int tt = 0; tt < 2; ++tt) {
          const f32x4 n0 = mfma(k10, as_frag(phq[u][hh][tt][1]), mfma(k00, as_frag(phq[u][hh][tt][0]), z));
          const f32x4 n1 = mfma(k11, as_frag(phq[u][hh][tt][1]), mfma(k01, as_frag(phq[u][hh][tt][0]), z));
          auto a_ = __builtin_amdgcn_permlane16_swap(__float_as_uint(n1[0]), __float_as_uint(n1[0]), false, false);
          auto b_ = __builtin_amdgcn_permlane32_swap(a_[1], a_[1], false, false);
          const float inv = __builtin_amdgcn_rcpf(__uint_as_float(b_[1]) + 1e-6f);
          const u32x4 of = scale_pack(n0, n1, inv);
          LAS char* ob = lds + 2 * SLOT + (2 * m + (n ^ tt)) * 7168;
          *(LAS u32x2*)(ob + o0) = u32x2{of[0], of[1]};
          if (g != 3) *(LAS u32x2*)(ob + o1) = u32x2{of[2], of[3]};
        }
      }
    BARRIER();
#pragma unroll
    for (int i = 0; i < 7; ++i) {
      const u32x4 v = *(LAS u32x4*)(lds + 2 * SLOT + w * 7168 + i * 1024 + lane * 16);
      st128(v, sW, lane * 16, WS_HB + ((rg * 8 + w) * 70 + 56 + 7 * ch + i) * 1024, 16);
    }
    WAITV(0);
    BARRIER();
    if (tid == 0) set_flag(ws, WS_FO + (rg * 16 + ch) * 8, epoch);
    STAMP(9);
    STAMP_FLUSH();
    return;
  }

  WAITV(0);
  BARRIER();
  if (tid == 0) set_flag(ws, WS_FH + ((rg * 2 + 1) * 16 + prod) * 8, epoch);
  issueD(9);
  const u32 rg5 = rh >> 1;
  const u32x4 xres = __builtin_amdgcn_raw_buffer_load_b128(sX, (8 * w + rho) * XS + 16 * cc, rh * 64 * XS + 128 * jj, 0);
  f32x4 acc[4][4] = {};
  u32 rb[4];
#pragma unroll
  for (int i = 0; i < 4; ++i) rb[i] = w * 2048 + (4 * g + q4) * 128 + ((i ^ sig) << 5) + 8 * p4;
  u32x4 A[9][4];
  auto loadA = [&](int i) __attribute__((always_inline)) {
#pragma unroll
    for (int tt = 0; tt < 4; ++tt) A[i][tt] = __builtin_amdgcn_raw_buffer_load_b128(sW, lane * 16, WS_HB + ((rh * 4 + tt) * 70 + w + 8 * i) * 1024, 16);
  };
  auto kstep = [&](int i) __attribute__((always_inline)) {
    LAS char* p0 = lds + ((25 + 2 * i) % 10) * SLOT; LAS char* p1 = lds + ((26 + 2 * i) % 10) * SLOT;
    bf16x8 B[4];
#pragma unroll
    for (int ct = 0; ct < 4; ++ct) B[ct] = trfrag(p0 + rb[ct], p1 + rb[ct]);
    if (i + 5 <= 8) { asm volatile("s_waitcnt lgkmcnt(0)" : "+v"(B[0]), "+v"(B[1]), "+v"(B[2]), "+v"(B[3]) :: "memory"); issueD(2 * i + 10); issueD(2 * i + 11); }
#pragma unroll
    for (int tt = 0; tt < 4; ++tt)
#pragma unroll
      for (int ct = 0; ct < 4; ++ct) acc[tt][ct] = mfma(B[ct], as_frag(A[i][tt]), acc[tt][ct]);
  };
  wait_flags(ws, WS_FH + (rg5 * 2) * 128, 14, epoch, lane);
  STAMP(8);
  loadA(0); loadA(1); loadA(2);
  wait_flags(ws, WS_FH + (rg5 * 2 + 1) * 128, 14, epoch, lane);
  STAMP(9);
  loadA(3); loadA(4); loadA(5); loadA(6);
  kstep(0); kstep(1); kstep(2);
  wait_flags(ws, WS_FO + rg5 * 128, 2, epoch, lane);
  STAMP(10);
  loadA(7); if (w < 6) loadA(8);
  kstep(3); kstep(4); kstep(5); kstep(6);
  WAITV(0);
  kstep(7); if (w < 6) kstep(8);
  STAMP(11);
  asm volatile("s_waitcnt lgkmcnt(0)" ::: "memory");
#pragma unroll
  for (int tt = 0; tt < 4; ++tt)
#pragma unroll
    for (int ct = 0; ct < 4; ++ct)
      *(LAS f32x4*)(lds + (2 * tt + (c16 >> 3)) * SLOT + w * 2048 + (c16 & 7) * 256 + (((4 * ct + g) ^ (c16 & 7)) << 4)) = acc[tt][ct];
  BARRIER();
  f32x4 r0, r1;
#pragma unroll
  for (int i = 0; i < 4; ++i) {
    const u32 xv = xres[i];
    const float lo = __uint_as_float(xv << 16), hi = __uint_as_float(xv & 0xffff0000u);
    if (i < 2) { r0[2 * i] = lo; r0[2 * i + 1] = hi; } else { r1[2 * i - 4] = lo; r1[2 * i - 3] = hi; }
  }
#pragma unroll
  for (int j = 0; j < 8; ++j) {
    LAS char* p = lds + w * SLOT + j * 2048 + rho * 256;
    r0 += *(LAS f32x4*)(p + (((2 * cc) ^ rho) << 4)); r1 += *(LAS f32x4*)(p + (((2 * cc + 1) ^ rho) << 4));
  }
  const srd_t sOut = mk_srd(out, S_ * XS);
  st128p(scale_pack(r0, r1, 1.f), sOut, (8 * w + rho) * XS + 16 * cc, rh * 64 * XS + 128 * jj, 0);
  if (b == 0 && tid == 0) *(u64*)(ws + WS_EPOCH) = epoch;
  STAMP(12);
  STAMP_FLUSH();
}

__global__ __launch_bounds__(512, 2) void fused_kernel(const char* __restrict__ x, const char* __restrict__ wqkv, const char* __restrict__ wphi,
    const char* __restrict__ wo, const char* __restrict__ wg, const char* __restrict__ wu, const char* __restrict__ wd, char* __restrict__ out, char* ws) {
  if (((blockIdx.x >> 3) & 7) == 7) body<true>(x, wqkv, wphi, wo, wg, wu, wd, out, ws);
  else body<false>(x, wqkv, wphi, wo, wg, wu, wd, out, ws);
}

extern "C" void kernel_launch(void* const* d_in, const int* in_sizes, int n_in, void* d_out, int out_size, void* d_ws, size_t ws_size, hipStream_t stream) {
  (void)in_sizes; (void)n_in; (void)out_size; (void)ws_size;
  (void)hipFuncSetAttribute((const void*)fused_kernel, hipFuncAttributeMaxDynamicSharedMemorySize, 163840);
  fused_kernel<<<256, 512, 163840, stream>>>((const char*)d_in[0], (const char*)d_in[1], (const char*)d_in[2], (const char*)d_in[3],
      (const char*)d_in[5], (const char*)d_in[6], (const char*)d_in[7], (char*)d_out, (char*)d_ws);
}
```

```cpp
#include <hip/hip_runtime.h>
#include <stdint.h>

typedef unsigned u32;
typedef unsigned long long u64;
typedef short bf16x8 __attribute__((ext_vector_type(8)));
typedef short s16x4 __attribute__((ext_vector_type(4)));
typedef float f32x4 __attribute__((ext_vector_type(4)));
typedef float f32x2 __attribute__((ext_vector_type(2)));
typedef u32 u32x4 __attribute__((ext_vector_type(4)));
typedef u32 u32x2 __attribute__((ext_vector_type(2)));
typedef __bf16 bf16x2 __attribute__((ext_vector_type(2)));
typedef __amdgpu_buffer_rsrc_t srd_t;
#define LAS __attribute__((address_space(3)))
#define GAS __attribute__((address_space(1)))
#define DI __device__ __forceinline__

constexpr int S_ = 2048, D_ = 448, F_ = 1792;
constexpr u32 XS = 896, QS = 2688, GS = 3584;
constexpr u32 SLOT = 16384;
constexpr u32 SC_V = 9 * SLOT, SC_WP = SC_V + 8192, SC_CS = SC_V + 10240, SC_DUMP = SC_V + 12800;
constexpr u32 WS_EPOCH = 0, WS_FP = 1024, WS_FH = 4096, WS_FR = 8192, WS_FO = 10240, WS_KVF = 16384,
              WS_PART = 131072, WS_QB = 4194304, WS_HB = 6291456, WS_END = WS_HB + 128u * 72u * 1024u;
constexpr int SPIN_CAP = 1 << 15;
constexpr float LOG2E = 1.4426950408889634f;

DI u32 rfl(u32 x) { return __builtin_amdgcn_readfirstlane(x); }
DI srd_t mk_srd(const void* p, u32 bytes) { return __builtin_amdgcn_make_buffer_rsrc((void*)p, (short)0, (int)bytes, 0x00020000); }
#define WAITV(n) asm volatile("s_waitcnt vmcnt(" #n ")" ::: "memory")
#define BARRIER() asm volatile("s_waitcnt lgkmcnt(0)\n\ts_barrier" ::: "memory")
#define WAITL() __builtin_amdgcn_s_waitcnt(0xC07F)
#define BARRIER_RAW() asm volatile("s_barrier" ::: "memory")
#define SB __builtin_amdgcn_sched_barrier(0)
DI f32x4 mfma(bf16x8 a, bf16x8 b, f32x4 c) { return __builtin_amdgcn_mfma_f32_16x16x32_bf16(a, b, c, 0, 0, 0); }
DI bf16x8 trfrag(LAS char* p0, LAS char* p1) {
  s16x4 lo = __builtin_amdgcn_ds_read_tr16_b64_v4i16((LAS s16x4*)p0);
  s16x4 hi = __builtin_amdgcn_ds_read_tr16_b64_v4i16((LAS s16x4*)p1);
  return __builtin_shufflevector(lo, hi, 0, 1, 2, 3, 4, 5, 6, 7);
}
DI u32 pk2(float a, float b) { f32x2 f = {a, b}; return __builtin_bit_cast(u32, __builtin_convertvector(f, bf16x2)); }
DI float rbf(float a) { return __uint_as_float(pk2(a, a) << 16); }
DI bf16x8 as_frag(u32x4 v) { return __builtin_bit_cast(bf16x8, v); }
DI u32 fu(float x) { return __float_as_uint(x); }
DI float uf(u32 x) { return __uint_as_float(x); }
DI float relu(float a) { float r; asm("v_max_f32 %0, 0, %1" : "=v"(r) : "v"(a)); return r; }
#define PAIR(OP, MID) "s_nop 1\n\tv_permlane16_swap_b32 %0, %1\n\t" OP " %0, %0, %1\n\tv_mov_b32 %1, %0\n\ts_nop 1\n\tv_permlane32_swap_b32 %0, %1\n\t" \
    OP " %0, %0, %1\n\t" MID "v_mov_b32 %1, %0\n\ts_nop 1\n\tv_permlane16_swap_b32 %0, %1"
template <class H>
DI void dual_exp(f32x4 p0, f32x4 p1, f32x4& ep0, f32x4& ep1, f32x4& em0, f32x4& em1, H hook, int h0) {
  float mx, mn;
  asm volatile("s_nop 7\n\tv_max_f32 %0, %2, %3\n\tv_max_f32 %1, -%2, -%3\n\tv_max3_f32 %0, %0, %4, %5\n\tv_max3_f32 %1, %1, -%4, -%5\n\t"
      "v_max3_f32 %0, %0, %6, %7\n\tv_max3_f32 %1, %1, -%6, -%7\n\tv_max3_f32 %0, %0, %8, %9\n\tv_max3_f32 %1, %1, -%8, -%9\n\t" PAIR("v_max_f32", "")
      : "=&v"(mx), "=&v"(mn) : "v"(p0[0]), "v"(p0[1]), "v"(p0[2]), "v"(p0[3]), "v"(p1[0]), "v"(p1[1]), "v"(p1[2]), "v"(p1[3]));
  hook(h0);
  asm volatile("" : "+v"(mx));
#pragma unroll
  for (int i = 0; i < 4; ++i) { ep0[i] = __builtin_amdgcn_exp2f(p0[i] - mx); ep1[i] = __builtin_amdgcn_exp2f(p1[i] - mx); }
  asm volatile("" : "+v"(ep0), "+v"(ep1));
  hook(h0 + 1);
  asm volatile("" : "+v"(mn));
  const f32x4 b0 = p0 + mn, b1 = p1 + mn;
#pragma unroll
  for (int i = 0; i < 4; ++i) { em0[i] = __builtin_amdgcn_exp2f(-b0[i]); em1[i] = __builtin_amdgcn_exp2f(-b1[i]); }
  asm volatile("" : "+v"(em0), "+v"(em1));
  hook(h0 + 2);
}
DI u32x2 pack4(f32x4 a) { return u32x2{pk2(a[0], a[1]), pk2(a[2], a[3])}; }
DI u32x4 pack8(f32x4 a, f32x4 b) { return u32x4{pk2(a[0], a[1]), pk2(a[2], a[3]), pk2(b[0], b[1]), pk2(b[2], b[3])}; }
DI f32x4 fix4(f32x4 a, f32x4 cs, float nm) { return __builtin_elementwise_fma(cs, f32x4{nm, nm, nm, nm}, a); }
DI void wait_flags(const char* ws, u32 off, u32 mask, u64 epoch) {
  const GAS u64* p = (const GAS u64*)(ws + off);
#pragma unroll 1
  for (int spin = 0; spin < SPIN_CAP; ++spin) {
    const u64 v = __hip_atomic_load(p, __ATOMIC_RELAXED, __HIP_MEMORY_SCOPE_AGENT);
    if ((~(u32)__ballot(v == epoch) & mask) == 0) break;
    __builtin_amdgcn_s_sleep(2);
  }
  __builtin_amdgcn_fence(__ATOMIC_ACQUIRE, "wavefront");
}
DI void st128(u32x4 v, srd_t s, u32 voff) { __builtin_amdgcn_raw_buffer_store_b128(v, s, voff, 0, 16); }

#define KC(i) ((kw >> (4 * (i))) & 15)
#define PIT(c) ((((c) < 6 ? pw0 >> (4 * (c)) : (c) < 11 ? pw1 >> (4 * ((c) - 6)) : (c) < 14 ? pw2 >> (4 * ((c) - 11)) : pw2 >> (4 * ((c) - 10)))) & 15)
#define DMA0(s, a, ck, c, v, srd) asm volatile("s_add_u32 m0, %1, %2\n\ts_mul_i32 %0, %3, %4\n\tbuffer_load_dwordx4 %5, %6, %0 offen lds" \
    : "=&s"(dso) : "s"(wb2), "n"((s) * SLOT), "s"(a), ck(c), "v"(v), "s"(srd) : "scc", "memory")
#define DMA1(v, srd) asm volatile("buffer_load_dwordx4 %1, %2, %0 offen offset:1024 lds" : "+s"(dso) : "v"(v), "s"(srd) : "memory")
#define ISS0(k, i, s) ISS0_##k(i, s)
#define ISS1(k, i, s) ISS1_##k(i, s)
#define ISS0_X(i, s) DMA0(s, KC(i), "n", 128, vx0, sX)
#define ISS1_X(i, s) DMA1(vx1, sX)
#define ISS0_U(i, s) DMA0(s, KC(i), "s", ustr, vU, sU0a)
#define ISS1_U(i, s) DMA1(vU, sU0b)
#define ISS0_V(i, s) DMA0(s, KC(i), "n", 64 * GS, vV, sG)
#define ISS1_V(i, s) DMA1(vV, sU)
#define ISS0_K(i, s) DMA0(s, KC(2 * (i)), "n", 64 * QS, vk, sQ)
#define ISS1_K(i, s) do { if ((i) < 3) asm volatile("s_mul_i32 %0, %1, %2\n\tbuffer_load_dwordx4 %3, %4, %0 offen offset:1024 lds" \
      : "=&s"(dso) : "s"(KC((2 * (i) + 1) % 7)), "n"(64 * QS), "v"(vk), "s"(sQb) : "memory"); \
    else asm volatile("s_add_u32 m0, %0, %1\n\ts_nop 0\n\tbuffer_load_dwordx4 %2, %3, %0 offen lds" :: "s"((w & 1) * 1024), "n"(SC_WP), "v"(l16), "s"(sP) : "scc", "memory"); } while (0)
#define ISS0_P(i, s) DMA0(s, PIT(i), "n", ((i) < 14 ? 64 : 112) * XS, vD[(i) < 14 ? 0 : 2], (i) < 14 ? sD : sO)
#define ISS1_P(i, s) DMA1(vD[(i) < 14 ? 1 : 3], (i) < 14 ? sD : sO)
#define VXOR(d, c, v) asm volatile("v_xor_b32 %0, %1, %2" : "=v"(d) : "n"(c), "v"(v))
#define SETADK(win) do { VXOR(ad[4], win, bK); VXOR(ad[5], (win) | 32, bK); } while (0)
#define SETADU(h, win) do { VXOR(ad[h], win, (h) ? bB : bA); VXOR(ad[(h) + 1], (win) | 32, (h) ? bB : bA); } while (0)
#define SETADV(h, win) do { VXOR(ad[h], (win) | ((h) << 9), bV); VXOR(ad[(h) + 1], (win) | ((h) << 9) | 32, bV); } while (0)
#define SETADP(h, win, sw) do { VXOR(ad[h], (win) | ((h) << 5), (sw) ? bP : bO); VXOR(ad[4 + (h)], (win) | ((h) << 5) | 2048, (sw) ? bP : bO); } while (0)
#define MFMA0(d, a, b) asm volatile("v_mfma_f32_16x16x32_bf16 %0, %1, %2, 0" : "=&v"(d) : "v"(a), "v"(b))
#define MFMA1(d, a, b) asm volatile("v_mfma_f32_16x16x32_bf16 %0, %1, %2, %0" : "+v"(d) : "v"(a), "v"(b))
#define MFMAZ(z, d, a, b) do { if (z) MFMA0(d, a, b); else MFMA1(d, a, b); } while (0)
#define RD_U(fb, i, off) F[fb][i] = trfrag(lds + ad[i] + (off), lds + ad[i] + (off) + 512)
#define RHALF(fb, i, h, a) do { const s16x4 t_ = __builtin_amdgcn_ds_read_tr16_b64_v4i16((LAS s16x4*)(lds + a)); \
    if (h) F[fb][i].hi = t_; else F[fb][i].lo = t_; } while (0)
#define RH_U(fb, i, off, h) RHALF(fb, i, h, ad[i] + (off) + 512 * (h))
#define RH_K(fb, i, off, h) RHALF(fb, i, h, ad[4 + ((i) & 1)] + (off) + ((i) >> 1) * 8192 + 512 * (h))
#define RH_P(fb, i, off, h) RHALF(fb, i, h, ad[4 * (h) + (i)] + (off))
#define MM_U(fb, i, ks, t) MFMAZ((ks) == 0, acc[t][i], F[fb][i], ((t) ? xb : xa)[ks])
#define MM_K(fb, i, ks, t) MFMAZ((ks) + ((i) >> 1) == 0, ack[t][(i) & 1], F[fb][i], ((t) ? xb : xa)[(ks) + ((i) >> 1)])
#define MM_P(fb, i, c, t) MFMAZ((c) == 0, ap[t][i], F[fb][i], A[c][t])
#define CSUM(d, fb, i, z) MFMAZ(z, d, F[fb][i], ones)
#define CS_OUT(k, d) do { asm volatile("s_nop 7" : "+v"(d)); *(LAS f32x4*)(lds + csad + (k) * 512) = d; } while (0)
#define CS_IN(k, i) (*(LAS f32x4*)(lds + (csad ^ ((i) * 64)) + (k) * 512))
#define XRD(j, s, e) do { xa[2 * (j) + (e)] = *(LAS u32x4*)(lds + (s) * SLOT + xr[e]); xb[2 * (j) + (e)] = *(LAS u32x4*)(lds + (s) * SLOT + xr[2 + (e)]); } while (0)
#define STAT1(ks) MFMAZ((ks) == 0, s1a, ones, xa[ks])
#define STAT2(ks) MFMAZ((ks) == 0, s2a, xa[ks], xa[ks])
#define STATS_OUT() stats_out()
#define STATS_IN() do { const f32x4 st_ = *(LAS f32x4*)(lds + stad); const f32x2 su_ = *(LAS f32x2*)(lds + (stad ^ 256)); \
    nma = st_[0]; rsa = st_[1]; sd2 = st_[2]; nmb = su_[0]; rsb = su_[1]; } while (0)
#define LDA0(c) asm volatile("s_lshl_b32 %1, %4, %5\n\tbuffer_load_dwordx4 %0, %2, %3, %1 offen sc1 ; LDA" : "=v"(A[c][0]), "=&s"(aso) \
    : "v"((c) < 14 ? vA : vAo), "s"(sW), "s"(PIT(c)), "n"((c) < 14 ? 12 : 13) : "scc", "memory")
#define LDA1(c) asm volatile("buffer_load_dwordx4 %0, %1, %2, %3 offen offset:1024 sc1 ; LDA" : "=v"(A[c][1]) : "v"((c) < 14 ? vA : vAo), "s"(sW), "s"(aso) : "memory")
#define PINA(c) asm volatile("; PINA %0 %1" : "+v"(A[c][0]), "+v"(A[c][1]))
#define POLL(off) if (w == 0) asm volatile("buffer_load_dwordx2 %0, %1, %2, %3 offen sc1 ; LDA" : "=v"(pv) : "v"(c16 * 8), "s"(sW), "s"(off) : "memory")
#define CHECK(off, mask, n) do { if (w != 0) break; WAITV(n); asm volatile("; PINA %0" : "+v"(pv)); \
    if ((~(u32)__ballot(__builtin_bit_cast(u64, pv) == epoch) & (mask)) != 0) wait_flags(ws, (off) + c16 * 8, mask, epoch); } while (0)
#define POLLR(off) if (w == 1) asm volatile("buffer_load_dwordx2 %0, %1, %2, %3 offen sc1 ; LDA" : "=v"(pv) : "v"(c16 * 8), "s"(sW), "s"(off) : "memory")
#define CHECKR(off, n) do { if (w != 1) break; WAITV(n); asm volatile("; PINA %0" : "+v"(pv)); \
    if ((~(u32)__ballot(__builtin_bit_cast(u64, pv) == epoch) & 0xffff) != 0) wait_flags(ws, (off) + c16 * 8, 0xffff, epoch); } while (0)
#define LDQ() asm volatile("buffer_load_dwordx4 %0, %1, %2, %3 offen sc1 ; LDA" : "=v"(qf) : "v"(l16), "s"(sW), "s"(WS_QB + ((rg * 8 + 2 * m + n) * 16 + head) * 1024) : "memory")
#define SETFLAG(off) __builtin_amdgcn_raw_buffer_store_b64(__builtin_bit_cast(u32x2, epoch), sW, (lane << 26) + (off), 0, 16)
#define FLAG_H(u) do { if (w == 2) SETFLAG(WS_FH + ((rg * 2 + (u)) * 16 + prod) * 8); } while (0)
#define FLAG_P() do { if (w == 2) SETFLAG(WS_FP + (head * 16 + rg) * 8); } while (0)
#define FLAG_O() do { if (w == 2) SETFLAG(WS_FO + (rg * 16 + head) * 8); } while (0)
#define FLAG_R(n) do { if (w != 1) break; WAITV(n); SETFLAG(WS_FR + (head * 16 + rg) * 8); } while (0)
#define XRES() asm volatile("buffer_load_dwordx4 %0, %1, %2, %3 offen ; LDA" : "=v"(xres) : "v"(vo), "s"(sX), "s"(soo) : "memory")
#define KV_XCHG(s) kv_xchg(s)
#define KV_RD(s, k) do { fa[k] = trfrag(lds + (kra + (s) * SLOT) + (k) * 4096, lds + (kra + (s) * SLOT) + (k) * 4096 + 512); \
                         fb[k] = trfrag(lds + krb + (k) * 2048, lds + krb + (k) * 2048 + 256); } while (0)
#define KV_MM(k) MFMAZ((k) == 0, csk, fa[k], fb[k])
#define KV_ST() do { asm volatile("s_nop 7" : "+v"(csk)); st128(__builtin_bit_cast(u32x4, csk), sW, l16 + WS_PART + (head * 16 + rg) * 8192 + wb1); } while (0)
#define RED_LOAD4(i) if (w == 1) { _Pragma("unroll") for (int i_ = i; i_ < (i) + 4; ++i_) \
    asm volatile("buffer_load_dwordx2 %0, %1, %2, %3 offen sc1 ; LDA" : "=v"(part[i_]) : "v"(lane * 8), "s"(sW), "s"(sofR + i_ * 8192) : "memory"); }
#define RED_STORE(n) do { if (w != 1) break; WAITV(n); red_store(); } while (0)
#define LDKV() asm volatile("buffer_load_dwordx2 %0, %1, %2, %3 offen sc1 ; LDA" : "=v"(kvp) : "v"(lane * 8), "s"(sW), "s"(WS_KVF + head * 4096 + w * 512) : "memory")
#define CODE_PF() do { u64 pc_; u32 len_; asm volatile("kcode_begin:\n\ts_getpc_b64 %0\n\ts_mov_b32 %1, kcode_end-kcode_begin" : "=s"(pc_), "=s"(len_)); \
    asm volatile("s_mov_b32 m0, %0\n\ts_nop 0\n\tbuffer_load_dword %1, %2, %3 offen lds" :: "n"(SC_PF), "v"(lane * 4), "s"(mk_srd((const void*)(pc_ & ~(u64)127), len_)), \
                 "s"(((b & ~7u) + w) * 256) : "memory"); } while (0)
#define MPF(i) asm volatile("s_mov_b32 m0, %0\n\ts_nop 0\n\tbuffer_load_dwordx4 %1, %2, 0 offen lds" :: "n"(SC_PF), \
    "v"(l16 + (b * 8 + w - ((i) ? 1568 : 0)) * 1024), "s"((i) ? sO : sD) : "memory")
#define KVF_SHARE(n) do { WAITV(n); asm volatile("; PINA %0" : "+v"(kvp)); *(LAS u32x2*)(lds + SC_V + w * 512 + lane * 8) = kvp; } while (0)
#define LEAVE() do { if (jj == 7) { WAITV(0); return; } } while (0)
#define FINAL_PREP() final_prep(FIN)
#define FINAL() final_out()
#define SETUP0() setup0()
#define SETUP1() setup1()
#define SETUP2() setup2()
#define SETUP3() setup3()
#define SETUP4() setup4()
#define PRE0() pre0()
#define PRE1() pre1()
#define EPOCH_LOAD() asm volatile("s_load_dwordx2 %0, %1, 0x0" : "=s"(epoch) : "s"(ws))
constexpr u32 SC_PF = SC_V + 15360;
constexpr u32 SC_ST0 = SC_V + 13312;
#define ROT (b&6)
#define SCHED \
  constexpr u32 FIN = 0 * SLOT; CODE_PF(); PRE0(); ISS0(X, 0, 0); ISS1(X, 0, 0); EPOCH_LOAD(); PRE1(); ISS0(U, 0, 1); ISS1(U, 0, 1); ISS0(X, 1, 2); \
  ISS1(X, 1, 2); ISS0(U, 1, 3); ISS1(U, 1, 3); ISS0(X, 2, 4); ISS1(X, 2, 4); SETUP0(); SETUP1(); SETADU(0, 0); SETADU(2, 0); WAITL(); WAITV(6); \
  BARRIER_RAW(); SB; \
  XRD(0, 0, 0); SB; XRD(0, 0, 1); SB; RD_U(1, 0, 16384); SB; RD_U(1, 1, 16384); SB; RD_U(1, 2, 16384); SB; RD_U(1, 3, 16384); SB; ISS0(U, 2, 5); SB; \
  ISS1(U, 2, 5); SB; SETUP2(); SB; CSUM(csa, 1, 0, 1); SB; WAITL(); MM_U(1, 0, 0, 0); SB; RH_U(0, 0, 24576, 0); SB; MM_U(1, 0, 0, 1); SB; \
  RH_U(0, 0, 24576, 1); SB; MM_U(1, 1, 0, 0); SB; RH_U(0, 1, 24576, 0); SB; MM_U(1, 1, 0, 1); SB; RH_U(0, 1, 24576, 1); SB; MM_U(1, 2, 0, 0); SB; \
  RH_U(0, 2, 24576, 0); SB; MM_U(1, 2, 0, 1); SB; RH_U(0, 2, 24576, 1); SB; ISS0(K, 0, 6); SB; MM_U(1, 3, 0, 0); SB; RH_U(0, 3, 24576, 0); SB; \
  MM_U(1, 3, 0, 1); SB; RH_U(0, 3, 24576, 1); SB; STAT1(0); SB; STAT2(0); SB; CSUM(csa, 0, 0, 0); SB; ISS1(K, 0, 6); SB; WAITL(); WAITV(6); \
  BARRIER_RAW(); SB; \
  MM_U(0, 0, 1, 0); SB; RH_U(1, 0, 49152, 0); SB; XRD(1, 2, 0); SB; MM_U(0, 0, 1, 1); SB; RH_U(1, 0, 49152, 1); SB; MM_U(0, 1, 1, 0); SB; \
  RH_U(1, 1, 49152, 0); SB; XRD(1, 2, 1); SB; MM_U(0, 1, 1, 1); SB; RH_U(1, 1, 49152, 1); SB; MM_U(0, 2, 1, 0); SB; RH_U(1, 2, 49152, 0); SB; \
  MM_U(0, 2, 1, 1); SB; RH_U(1, 2, 49152, 1); SB; ISS0(X, 3, 7); SB; MM_U(0, 3, 1, 0); SB; RH_U(1, 3, 49152, 0); SB; MM_U(0, 3, 1, 1); SB; \
  RH_U(1, 3, 49152, 1); SB; STAT1(1); SB; ISS1(X, 3, 7); SB; STAT2(1); SB; CSUM(csa, 1, 0, 0); SB; MM_U(1, 0, 2, 0); SB; RH_U(0, 0, 57344, 0); SB; \
  MM_U(1, 0, 2, 1); SB; RH_U(0, 0, 57344, 1); SB; MM_U(1, 1, 2, 0); SB; RH_U(0, 1, 57344, 0); SB; MM_U(1, 1, 2, 1); SB; RH_U(0, 1, 57344, 1); SB; \
  ISS0(K, 1, 8); SB; MM_U(1, 2, 2, 0); SB; RH_U(0, 2, 57344, 0); SB; MM_U(1, 2, 2, 1); SB; RH_U(0, 2, 57344, 1); SB; MM_U(1, 3, 2, 0); SB; \
  RH_U(0, 3, 57344, 0); SB; MM_U(1, 3, 2, 1); SB; RH_U(0, 3, 57344, 1); SB; ISS1(K, 1, 8); SB; STAT1(2); SB; STAT2(2); SB; CSUM(csa, 0, 0, 0); SB; \
  SETUP3(); SB; SETADU(0, 65536); SB; SETADU(2, 65536); SB; WAITL(); WAITV(6); BARRIER_RAW(); SB; \
  MM_U(0, 0, 3, 0); SB; RH_U(1, 0, 16384, 0); SB; XRD(2, 4, 0); SB; MM_U(0, 0, 3, 1); SB; RH_U(1, 0, 16384, 1); SB; MM_U(0, 1, 3, 0); SB; \
  RH_U(1, 1, 16384, 0); SB; XRD(2, 4, 1); SB; MM_U(0, 1, 3, 1); SB; RH_U(1, 1, 16384, 1); SB; MM_U(0, 2, 3, 0); SB; RH_U(1, 2, 16384, 0); SB; \
  MM_U(0, 2, 3, 1); SB; RH_U(1, 2, 16384, 1); SB; ISS0(X, 4, 0); SB; MM_U(0, 3, 3, 0); SB; RH_U(1, 3, 16384, 0); SB; MM_U(0, 3, 3, 1); SB; \
  RH_U(1, 3, 16384, 1); SB; STAT1(3); SB; ISS1(X, 4, 0); SB; STAT2(3); SB; CSUM(csa, 1, 0, 0); SB; MM_U(1, 0, 4, 0); SB; RH_U(0, 0, 24576, 0); SB; \
  MM_U(1, 0, 4, 1); SB; RH_U(0, 0, 24576, 1); SB; MM_U(1, 1, 4, 0); SB; RH_U(0, 1, 24576, 0); SB; MM_U(1, 1, 4, 1); SB; RH_U(0, 1, 24576, 1); SB; \
  ISS0(K, 2, 1); SB; MM_U(1, 2, 4, 0); SB; RH_U(0, 2, 24576, 0); SB; MM_U(1, 2, 4, 1); SB; RH_U(0, 2, 24576, 1); SB; MM_U(1, 3, 4, 0); SB; \
  RH_U(0, 3, 24576, 0); SB; MM_U(1, 3, 4, 1); SB; RH_U(0, 3, 24576, 1); SB; ISS1(K, 2, 1); SB; STAT1(4); SB; STAT2(4); SB; CSUM(csa, 0, 0, 0); SB; \
  SETUP4(); SB; SETADK(65536); SB; WAITL(); WAITV(6); BARRIER_RAW(); SB; \
  MM_U(0, 0, 5, 0); SB; RH_K(1, 0, 32768, 0); SB; XRD(3, 7, 0); SB; MM_U(0, 0, 5, 1); SB; RH_K(1, 0, 32768, 1); SB; MM_U(0, 1, 5, 0); SB; \
  RH_K(1, 1, 32768, 0); SB; XRD(3, 7, 1); SB; MM_U(0, 1, 5, 1); SB; RH_K(1, 1, 32768, 1); SB; MM_U(0, 2, 5, 0); SB; RH_K(1, 2, 32768, 0); SB; \
  MM_U(0, 2, 5, 1); SB; RH_K(1, 2, 32768, 1); SB; ISS0(X, 5, 2); SB; MM_U(0, 3, 5, 0); SB; RH_K(1, 3, 32768, 0); SB; MM_U(0, 3, 5, 1); SB; \
  RH_K(1, 3, 32768, 1); SB; ISS1(X, 5, 2); SB; STAT1(5); SB; STAT2(5); SB; MM_K(1, 0, 0, 0); SB; RH_K(0, 0, 33792, 0); SB; CSUM(csk, 1, 0, 1); SB; \
  RH_K(0, 0, 33792, 1); SB; MM_K(1, 0, 0, 1); SB; RH_K(0, 1, 33792, 0); SB; MM_K(1, 1, 0, 0); SB; RH_K(0, 1, 33792, 1); SB; MM_K(1, 1, 0, 1); SB; \
  RH_K(0, 2, 33792, 0); SB; MM_K(1, 2, 0, 0); SB; RH_K(0, 2, 33792, 1); SB; ISS0(K, 3, 3); SB; CSUM(csk, 1, 2, 0); SB; RH_K(0, 3, 33792, 0); SB; \
  MM_K(1, 2, 0, 1); SB; RH_K(0, 3, 33792, 1); SB; MM_K(1, 3, 0, 0); SB; MM_K(1, 3, 0, 1); SB; STAT1(6); SB; STAT2(6); SB; ISS1(K, 3, 3); SB; \
  SETADK(131072); SB; WAITV(6); BARRIER_RAW(); SB; \
  MM_K(0, 0, 2, 0); SB; RH_K(1, 0, 0, 0); SB; XRD(4, 0, 0); SB; CSUM(csk, 0, 0, 0); SB; RH_K(1, 0, 0, 1); SB; MM_K(0, 0, 2, 1); SB; \
  RH_K(1, 1, 0, 0); SB; MM_K(0, 1, 2, 0); SB; RH_K(1, 1, 0, 1); SB; XRD(4, 0, 1); SB; MM_K(0, 1, 2, 1); SB; RH_K(1, 2, 0, 0); SB; MM_K(0, 2, 2, 0); SB; \
  RH_K(1, 2, 0, 1); SB; CSUM(csk, 0, 2, 0); SB; RH_K(1, 3, 0, 0); SB; ISS0(X, 6, 4); SB; MM_K(0, 2, 2, 1); SB; RH_K(1, 3, 0, 1); SB; \
  MM_K(0, 3, 2, 0); SB; MM_K(0, 3, 2, 1); SB; ISS1(X, 6, 4); SB; STAT1(7); SB; STAT2(7); SB; MM_K(1, 0, 4, 0); SB; RH_K(0, 0, 1024, 0); SB; \
  CSUM(csk, 1, 0, 0); SB; RH_K(0, 0, 1024, 1); SB; MM_K(1, 0, 4, 1); SB; RH_K(0, 1, 1024, 0); SB; MM_K(1, 1, 4, 0); SB; RH_K(0, 1, 1024, 1); SB; \
  MM_K(1, 1, 4, 1); SB; RH_K(0, 2, 1024, 0); SB; MM_K(1, 2, 4, 0); SB; RH_K(0, 2, 1024, 1); SB; ISS0(U, 3, 5); SB; CSUM(csk, 1, 2, 0); SB; \
  RH_K(0, 3, 1024, 0); SB; MM_K(1, 2, 4, 1); SB; RH_K(0, 3, 1024, 1); SB; MM_K(1, 3, 4, 0); SB; MM_K(1, 3, 4, 1); SB; STAT1(8); SB; STAT2(8); SB; \
  ISS1(U, 3, 5); SB; SETADK(0); SB; WAITV(6); BARRIER_RAW(); SB; \
  MM_K(0, 0, 6, 0); SB; RH_K(1, 0, 16384, 0); SB; XRD(5, 2, 0); SB; CSUM(csk, 0, 0, 0); SB; RH_K(1, 0, 16384, 1); SB; MM_K(0, 0, 6, 1); SB; \
  RH_K(1, 1, 16384, 0); SB; MM_K(0, 1, 6, 0); SB; RH_K(1, 1, 16384, 1); SB; XRD(5, 2, 1); SB; MM_K(0, 1, 6, 1); SB; RH_K(1, 2, 16384, 0); SB; \
  MM_K(0, 2, 6, 0); SB; RH_K(1, 2, 16384, 1); SB; CSUM(csk, 0, 2, 0); SB; RH_K(1, 3, 16384, 0); SB; ISS0(U, 4, 7); SB; MM_K(0, 2, 6, 1); SB; \
  RH_K(1, 3, 16384, 1); SB; MM_K(0, 3, 6, 0); SB; MM_K(0, 3, 6, 1); SB; ISS1(U, 4, 7); SB; STAT1(9); SB; STAT2(9); SB; MM_K(1, 0, 8, 0); SB; \
  RH_K(0, 0, 17408, 0); SB; CSUM(csk, 1, 0, 0); SB; RH_K(0, 0, 17408, 1); SB; MM_K(1, 0, 8, 1); SB; RH_K(0, 1, 17408, 0); SB; MM_K(1, 1, 8, 0); SB; \
  RH_K(0, 1, 17408, 1); SB; ISS0(U, 5, 6); SB; MM_K(1, 1, 8, 1); SB; RH_K(0, 2, 17408, 0); SB; MM_K(1, 2, 8, 0); SB; RH_K(0, 2, 17408, 1); SB; \
  CSUM(csk, 1, 2, 0); SB; RH_K(0, 3, 17408, 0); SB; MM_K(1, 2, 8, 1); SB; RH_K(0, 3, 17408, 1); SB; ISS1(U, 5, 6); SB; MM_K(1, 3, 8, 0); SB; \
  MM_K(1, 3, 8, 1); SB; STAT1(10); SB; STAT2(10); SB; MPF(0); SB; WAITV(5); BARRIER_RAW(); SB; \
  MM_K(0, 0, 10, 0); SB; RH_K(1, 0, 49152, 0); SB; XRD(6, 4, 0); SB; CSUM(csk, 0, 0, 0); SB; RH_K(1, 0, 49152, 1); SB; MM_K(0, 0, 10, 1); SB; \
  RH_K(1, 1, 49152, 0); SB; MM_K(0, 1, 10, 0); SB; RH_K(1, 1, 49152, 1); SB; XRD(6, 4, 1); SB; MM_K(0, 1, 10, 1); SB; RH_K(1, 2, 49152, 0); SB; \
  MM_K(0, 2, 10, 0); SB; RH_K(1, 2, 49152, 1); SB; CSUM(csk, 0, 2, 0); SB; RH_K(1, 3, 49152, 0); SB; ISS0(U, 6, 0); SB; MM_K(0, 2, 10, 1); SB; \
  RH_K(1, 3, 49152, 1); SB; MM_K(0, 3, 10, 0); SB; MM_K(0, 3, 10, 1); SB; ISS1(U, 6, 0); SB; STAT1(11); SB; STAT2(11); SB; MM_K(1, 0, 12, 0); SB; \
  RH_U(0, 0, 16384, 0); SB; CSUM(csk, 1, 0, 0); SB; RH_U(0, 0, 16384, 1); SB; MM_K(1, 0, 12, 1); SB; RH_U(0, 1, 16384, 0); SB; MM_K(1, 1, 12, 0); SB; \
  RH_U(0, 1, 16384, 1); SB; MM_K(1, 1, 12, 1); SB; RH_U(0, 2, 16384, 0); SB; ISS0(V, 0, 2); SB; MM_K(1, 2, 12, 0); SB; RH_U(0, 2, 16384, 1); SB; \
  CSUM(csk, 1, 2, 0); SB; RH_U(0, 3, 16384, 0); SB; MM_K(1, 2, 12, 1); SB; RH_U(0, 3, 16384, 1); SB; MM_K(1, 3, 12, 0); SB; MM_K(1, 3, 12, 1); SB; \
  ISS1(V, 0, 2); SB; STAT1(12); SB; STAT2(12); SB; STAT1(13); SB; STAT2(13); SB; CSUM(csa, 0, 0, 0); SB; MPF(1); SB; MM_U(0, 0, 6, 0); SB; \
  RH_U(1, 0, 24576, 0); SB; MM_U(0, 0, 6, 1); SB; RH_U(1, 0, 24576, 1); SB; MM_U(0, 1, 6, 0); SB; RH_U(1, 1, 24576, 0); SB; \
  CS_OUT(1, csk); KV_XCHG(8); SB; MM_U(0, 1, 6, 1); SB; RH_U(1, 1, 24576, 1); SB; MM_U(0, 2, 6, 0); SB; RH_U(1, 2, 24576, 0); SB; MM_U(0, 2, 6, 1); SB; \
  RH_U(1, 2, 24576, 1); SB; STATS_OUT(); SB; MM_U(0, 3, 6, 0); SB; RH_U(1, 3, 24576, 0); SB; MM_U(0, 3, 6, 1); SB; RH_U(1, 3, 24576, 1); SB; \
  CSUM(csa, 1, 0, 0); SB; kv_prep(8); SB; WAITL(); BARRIER_RAW(); SB; \
  STATS_IN(); \
  kv_feat(8, [&](int k_) __attribute__((always_inline)) { SB; if (k_ == 0) {  } if (k_ == 1) { ISS0(V, 1, 1); } if (k_ == 2) {  } if (k_ == 3) { ISS1(V, 1, 1); } if (k_ == 4) { ISS0(V, 2, 4); } if (k_ == 5) {  } if (k_ == 6) { ISS1(V, 2, 4); } if (k_ == 7) {  } if (k_ == 8) { ISS0(V, 3, 3); } if (k_ == 9) { ISS1(V, 3, 3); } SB; }); \
  WAITL(); WAITV(14); BARRIER_RAW(); SB; \
  MM_U(1, 0, 7, 0); SB; RH_U(0, 0, 49152, 0); SB; MM_U(1, 0, 7, 1); SB; RH_U(0, 0, 49152, 1); SB; KV_RD(8, 0); SB; MM_U(1, 1, 7, 0); SB; \
  RH_U(0, 1, 49152, 0); SB; KV_RD(8, 1); SB; MM_U(1, 1, 7, 1); SB; RH_U(0, 1, 49152, 1); SB; MM_U(1, 2, 7, 0); SB; RH_U(0, 2, 49152, 0); SB; \
  KV_RD(8, 2); SB; MM_U(1, 2, 7, 1); SB; RH_U(0, 2, 49152, 1); SB; KV_RD(8, 3); SB; MM_U(1, 3, 7, 0); SB; RH_U(0, 3, 49152, 0); SB; \
  MM_U(1, 3, 7, 1); SB; RH_U(0, 3, 49152, 1); SB; ISS0(V, 4, 5); SB; CSUM(csa, 0, 0, 0); SB; ISS1(V, 4, 5); SB; \
  WAITL(); KV_MM(0); KV_MM(1); KV_MM(2); KV_MM(3); SB; MM_U(0, 0, 8, 0); SB; RH_U(1, 0, 57344, 0); SB; MM_U(0, 0, 8, 1); SB; RH_U(1, 0, 57344, 1); SB; \
  MM_U(0, 1, 8, 0); SB; RH_U(1, 1, 57344, 0); SB; MM_U(0, 1, 8, 1); SB; RH_U(1, 1, 57344, 1); SB; MM_U(0, 2, 8, 0); SB; RH_U(1, 2, 57344, 0); SB; \
  MM_U(0, 2, 8, 1); SB; RH_U(1, 2, 57344, 1); SB; MM_U(0, 3, 8, 0); SB; RH_U(1, 3, 57344, 0); SB; MM_U(0, 3, 8, 1); SB; RH_U(1, 3, 57344, 1); SB; \
  CSUM(csa, 1, 0, 0); SB; KV_ST(); SB; WAITV(15); BARRIER_RAW(); SB; \
  MM_U(1, 0, 9, 0); SB; RH_U(0, 0, 32768, 0); SB; MM_U(1, 0, 9, 1); SB; RH_U(0, 0, 32768, 1); SB; MM_U(1, 1, 9, 0); SB; RH_U(0, 1, 32768, 0); SB; \
  MM_U(1, 1, 9, 1); SB; RH_U(0, 1, 32768, 1); SB; MM_U(1, 2, 9, 0); SB; RH_U(0, 2, 32768, 0); SB; MM_U(1, 2, 9, 1); SB; RH_U(0, 2, 32768, 1); SB; \
  MM_U(1, 3, 9, 0); SB; RH_U(0, 3, 32768, 0); SB; MM_U(1, 3, 9, 1); SB; RH_U(0, 3, 32768, 1); SB; CSUM(csa, 0, 0, 0); SB; MM_U(0, 0, 10, 0); SB; \
  RH_U(1, 0, 40960, 0); SB; MM_U(0, 0, 10, 1); SB; RH_U(1, 0, 40960, 1); SB; MM_U(0, 1, 10, 0); SB; RH_U(1, 1, 40960, 0); SB; MM_U(0, 1, 10, 1); SB; \
  RH_U(1, 1, 40960, 1); SB; MM_U(0, 2, 10, 0); SB; RH_U(1, 2, 40960, 0); SB; MM_U(0, 2, 10, 1); SB; RH_U(1, 2, 40960, 1); SB; MM_U(0, 3, 10, 0); SB; \
  RH_U(1, 3, 40960, 0); SB; MM_U(0, 3, 10, 1); SB; RH_U(1, 3, 40960, 1); SB; CSUM(csa, 1, 0, 0); SB; SETADU(0, 0); SB; SETADU(2, 0); SB; WAITV(0); \
  BARRIER_RAW(); SB; \
  FLAG_P(); MM_U(1, 0, 11, 0); SB; RH_U(0, 0, 0, 0); SB; MM_U(1, 0, 11, 1); SB; RH_U(0, 0, 0, 1); SB; MM_U(1, 1, 11, 0); SB; RH_U(0, 1, 0, 0); SB; \
  MM_U(1, 1, 11, 1); SB; RH_U(0, 1, 0, 1); SB; MM_U(1, 2, 11, 0); SB; RH_U(0, 2, 0, 0); SB; ISS0(V, 5, 8); SB; MM_U(1, 2, 11, 1); SB; \
  RH_U(0, 2, 0, 1); SB; MM_U(1, 3, 11, 0); SB; RH_U(0, 3, 0, 0); SB; MM_U(1, 3, 11, 1); SB; RH_U(0, 3, 0, 1); SB; CSUM(csa, 0, 0, 0); SB; \
  ISS1(V, 5, 8); SB; MM_U(0, 0, 12, 0); SB; RH_U(1, 0, 8192, 0); SB; MM_U(0, 0, 12, 1); SB; RH_U(1, 0, 8192, 1); SB; MM_U(0, 1, 12, 0); SB; \
  RH_U(1, 1, 8192, 0); SB; MM_U(0, 1, 12, 1); SB; RH_U(1, 1, 8192, 1); SB; MM_U(0, 2, 12, 0); SB; RH_U(1, 2, 8192, 0); SB; ISS0(V, 6, 7); SB; \
  MM_U(0, 2, 12, 1); SB; RH_U(1, 2, 8192, 1); SB; MM_U(0, 3, 12, 0); SB; RH_U(1, 3, 8192, 0); SB; MM_U(0, 3, 12, 1); SB; RH_U(1, 3, 8192, 1); SB; \
  CSUM(csa, 1, 0, 0); SB; ISS1(V, 6, 7); SB; SETADV(0, 0); SB; SETADV(2, 0); SB; CS_OUT(0, csa); WAITL(); WAITV(14); BARRIER_RAW(); SB; \
  MM_U(1, 0, 13, 0); SB; RH_U(0, 0, 32768, 0); SB; MM_U(1, 0, 13, 1); SB; RH_U(0, 0, 32768, 1); SB; MM_U(1, 1, 13, 0); SB; RH_U(0, 1, 32768, 0); SB; \
  MM_U(1, 1, 13, 1); SB; RH_U(0, 1, 32768, 1); SB; MM_U(1, 2, 13, 0); SB; RH_U(0, 2, 32768, 0); SB; ISS0(P, 0, 6); SB; MM_U(1, 2, 13, 1); SB; \
  RH_U(0, 2, 32768, 1); SB; MM_U(1, 3, 13, 0); SB; RH_U(0, 3, 32768, 0); SB; MM_U(1, 3, 13, 1); SB; RH_U(0, 3, 32768, 1); SB; CSUM(csa, 0, 0, 1); SB; \
  ISS1(P, 0, 6); SB; epi_h(0, [](int) {}); MM_U(0, 0, 0, 0); SB; RH_U(1, 0, 40960, 0); SB; MM_U(0, 0, 0, 1); SB; RH_U(1, 0, 40960, 1); SB; \
  MM_U(0, 1, 0, 0); SB; RH_U(1, 1, 40960, 0); SB; MM_U(0, 1, 0, 1); SB; RH_U(1, 1, 40960, 1); SB; MM_U(0, 2, 0, 0); SB; RH_U(1, 2, 40960, 0); SB; \
  ISS0(P, 1, 0); SB; MM_U(0, 2, 0, 1); SB; RH_U(1, 2, 40960, 1); SB; MM_U(0, 3, 0, 0); SB; RH_U(1, 3, 40960, 0); SB; MM_U(0, 3, 0, 1); SB; \
  RH_U(1, 3, 40960, 1); SB; CSUM(csa, 1, 0, 0); SB; ISS1(P, 1, 0); SB; WAITV(17); BARRIER_RAW(); SB; \
  MM_U(1, 0, 1, 0); SB; RH_U(0, 0, 16384, 0); SB; MM_U(1, 0, 1, 1); SB; RH_U(0, 0, 16384, 1); SB; MM_U(1, 1, 1, 0); SB; RH_U(0, 1, 16384, 0); SB; \
  MM_U(1, 1, 1, 1); SB; RH_U(0, 1, 16384, 1); SB; MM_U(1, 2, 1, 0); SB; RH_U(0, 2, 16384, 0); SB; MM_U(1, 2, 1, 1); SB; RH_U(0, 2, 16384, 1); SB; \
  MM_U(1, 3, 1, 0); SB; RH_U(0, 3, 16384, 0); SB; MM_U(1, 3, 1, 1); SB; RH_U(0, 3, 16384, 1); SB; CSUM(csa, 0, 0, 0); SB; \
  POLLR(WS_FP + head * 128); SB; MM_U(0, 0, 2, 0); SB; RH_U(1, 0, 24576, 0); SB; MM_U(0, 0, 2, 1); SB; RH_U(1, 0, 24576, 1); SB; MM_U(0, 1, 2, 0); SB; \
  RH_U(1, 1, 24576, 0); SB; MM_U(0, 1, 2, 1); SB; RH_U(1, 1, 24576, 1); SB; MM_U(0, 2, 2, 0); SB; RH_U(1, 2, 24576, 0); SB; MM_U(0, 2, 2, 1); SB; \
  RH_U(1, 2, 24576, 1); SB; MM_U(0, 3, 2, 0); SB; RH_U(1, 3, 24576, 0); SB; MM_U(0, 3, 2, 1); SB; RH_U(1, 3, 24576, 1); SB; CSUM(csa, 1, 0, 0); SB; \
  SETADV(0, 65536); SB; SETADV(2, 65536); SB; CHECKR(WS_FP + head * 128, 0); WAITV(0); BARRIER_RAW(); SB; \
  FLAG_H(0); MM_U(1, 0, 3, 0); SB; RH_U(0, 0, 0, 0); SB; MM_U(1, 0, 3, 1); SB; RH_U(0, 0, 0, 1); SB; MM_U(1, 1, 3, 0); SB; RH_U(0, 1, 0, 0); SB; \
  RED_LOAD4(0); SB; MM_U(1, 1, 3, 1); SB; RH_U(0, 1, 0, 1); SB; MM_U(1, 2, 3, 0); SB; RH_U(0, 2, 0, 0); SB; MM_U(1, 2, 3, 1); SB; RH_U(0, 2, 0, 1); SB; \
  ISS0(P, 2, 2); SB; MM_U(1, 3, 3, 0); SB; RH_U(0, 3, 0, 0); SB; MM_U(1, 3, 3, 1); SB; RH_U(0, 3, 0, 1); SB; CSUM(csa, 0, 0, 0); SB; ISS1(P, 2, 2); SB; \
  MM_U(0, 0, 4, 0); SB; RH_U(1, 0, 8192, 0); SB; MM_U(0, 0, 4, 1); SB; RH_U(1, 0, 8192, 1); SB; MM_U(0, 1, 4, 0); SB; RH_U(1, 1, 8192, 0); SB; \
  MM_U(0, 1, 4, 1); SB; RH_U(1, 1, 8192, 1); SB; MM_U(0, 2, 4, 0); SB; RH_U(1, 2, 8192, 0); SB; MM_U(0, 2, 4, 1); SB; RH_U(1, 2, 8192, 1); SB; \
  MM_U(0, 3, 4, 0); SB; RH_U(1, 3, 8192, 0); SB; MM_U(0, 3, 4, 1); SB; RH_U(1, 3, 8192, 1); SB; CSUM(csa, 1, 0, 0); SB; RED_LOAD4(4); SB; \
  SETADV(0, 0); SB; SETADV(2, 0); SB; WAITV(15); BARRIER_RAW(); SB; \
  MM_U(1, 0, 5, 0); SB; RH_U(0, 0, 49152, 0); SB; MM_U(1, 0, 5, 1); SB; RH_U(0, 0, 49152, 1); SB; MM_U(1, 1, 5, 0); SB; RH_U(0, 1, 49152, 0); SB; \
  RED_LOAD4(8); SB; MM_U(1, 1, 5, 1); SB; RH_U(0, 1, 49152, 1); SB; MM_U(1, 2, 5, 0); SB; RH_U(0, 2, 49152, 0); SB; MM_U(1, 2, 5, 1); SB; \
  RH_U(0, 2, 49152, 1); SB; ISS0(P, 3, 1); SB; MM_U(1, 3, 5, 0); SB; RH_U(0, 3, 49152, 0); SB; MM_U(1, 3, 5, 1); SB; RH_U(0, 3, 49152, 1); SB; \
  CSUM(csa, 0, 0, 0); SB; ISS1(P, 3, 1); SB; MM_U(0, 0, 6, 0); SB; RH_U(1, 0, 57344, 0); SB; MM_U(0, 0, 6, 1); SB; RH_U(1, 0, 57344, 1); SB; \
  MM_U(0, 1, 6, 0); SB; RH_U(1, 1, 57344, 0); SB; MM_U(0, 1, 6, 1); SB; RH_U(1, 1, 57344, 1); SB; MM_U(0, 2, 6, 0); SB; RH_U(1, 2, 57344, 0); SB; \
  MM_U(0, 2, 6, 1); SB; RH_U(1, 2, 57344, 1); SB; MM_U(0, 3, 6, 0); SB; RH_U(1, 3, 57344, 0); SB; MM_U(0, 3, 6, 1); SB; RH_U(1, 3, 57344, 1); SB; \
  CSUM(csa, 1, 0, 0); SB; RED_LOAD4(12); SB; SETADV(0, 65536); SB; SETADV(2, 65536); SB; WAITV(15); BARRIER_RAW(); SB; \
  MM_U(1, 0, 7, 0); SB; RH_U(0, 0, 16384, 0); SB; MM_U(1, 0, 7, 1); SB; RH_U(0, 0, 16384, 1); SB; MM_U(1, 1, 7, 0); SB; RH_U(0, 1, 16384, 0); SB; \
  MM_U(1, 1, 7, 1); SB; RH_U(0, 1, 16384, 1); SB; MM_U(1, 2, 7, 0); SB; RH_U(0, 2, 16384, 0); SB; ISS0(P, 4, 4); SB; MM_U(1, 2, 7, 1); SB; \
  RH_U(0, 2, 16384, 1); SB; MM_U(1, 3, 7, 0); SB; RH_U(0, 3, 16384, 0); SB; MM_U(1, 3, 7, 1); SB; RH_U(0, 3, 16384, 1); SB; CSUM(csa, 0, 0, 0); SB; \
  ISS1(P, 4, 4); SB; MM_U(0, 0, 8, 0); SB; RH_U(1, 0, 24576, 0); SB; MM_U(0, 0, 8, 1); SB; RH_U(1, 0, 24576, 1); SB; MM_U(0, 1, 8, 0); SB; \
  RH_U(1, 1, 24576, 0); SB; MM_U(0, 1, 8, 1); SB; RH_U(1, 1, 24576, 1); SB; MM_U(0, 2, 8, 0); SB; RH_U(1, 2, 24576, 0); SB; MM_U(0, 2, 8, 1); SB; \
  RH_U(1, 2, 24576, 1); SB; MM_U(0, 3, 8, 0); SB; RH_U(1, 3, 24576, 0); SB; MM_U(0, 3, 8, 1); SB; RH_U(1, 3, 24576, 1); SB; CSUM(csa, 1, 0, 0); SB; \
  SETADV(0, 131072); SB; SETADV(2, 131072); SB; WAITV(14); BARRIER_RAW(); SB; \
  RED_STORE(2); MM_U(1, 0, 9, 0); SB; RH_U(0, 0, 0, 0); SB; MM_U(1, 0, 9, 1); SB; RH_U(0, 0, 0, 1); SB; MM_U(1, 1, 9, 0); SB; RH_U(0, 1, 0, 0); SB; \
  POLL(WS_FH + (rg * 2) * 128); SB; MM_U(1, 1, 9, 1); SB; RH_U(0, 1, 0, 1); SB; MM_U(1, 2, 9, 0); SB; RH_U(0, 2, 0, 0); SB; MM_U(1, 2, 9, 1); SB; \
  RH_U(0, 2, 0, 1); SB; ISS0(P, 5, 3); SB; MM_U(1, 3, 9, 0); SB; RH_U(0, 3, 0, 0); SB; MM_U(1, 3, 9, 1); SB; RH_U(0, 3, 0, 1); SB; \
  CSUM(csa, 0, 0, 0); SB; ISS1(P, 5, 3); SB; MM_U(0, 0, 10, 0); SB; RH_U(1, 0, 8192, 0); SB; MM_U(0, 0, 10, 1); SB; RH_U(1, 0, 8192, 1); SB; \
  MM_U(0, 1, 10, 0); SB; RH_U(1, 1, 8192, 0); SB; MM_U(0, 1, 10, 1); SB; RH_U(1, 1, 8192, 1); SB; MM_U(0, 2, 10, 0); SB; RH_U(1, 2, 8192, 0); SB; \
  MM_U(0, 2, 10, 1); SB; RH_U(1, 2, 8192, 1); SB; MM_U(0, 3, 10, 0); SB; RH_U(1, 3, 8192, 0); SB; MM_U(0, 3, 10, 1); SB; RH_U(1, 3, 8192, 1); SB; \
  CSUM(csa, 1, 0, 0); SB; SETADV(0, 65536); SB; SETADV(2, 65536); SB; CHECK(WS_FH + (rg * 2) * 128, 0xffff, 2); WAITV(14); BARRIER_RAW(); SB; \
  FLAG_R(0); MM_U(1, 0, 11, 0); SB; RH_U(0, 0, 49152, 0); SB; MM_U(1, 0, 11, 1); SB; RH_U(0, 0, 49152, 1); SB; LDQ(); SB; MM_U(1, 1, 11, 0); SB; \
  RH_U(0, 1, 49152, 0); SB; LDA0(0); SB; MM_U(1, 1, 11, 1); SB; RH_U(0, 1, 49152, 1); SB; MM_U(1, 2, 11, 0); SB; RH_U(0, 2, 49152, 0); SB; LDA1(0); SB; \
  MM_U(1, 2, 11, 1); SB; RH_U(0, 2, 49152, 1); SB; LDA0(1); SB; MM_U(1, 3, 11, 0); SB; RH_U(0, 3, 49152, 0); SB; MM_U(1, 3, 11, 1); SB; \
  RH_U(0, 3, 49152, 1); SB; ISS0(P, 6, 5); SB; CSUM(csa, 0, 0, 0); SB; ISS1(P, 6, 5); SB; MM_U(0, 0, 12, 0); SB; RH_U(1, 0, 57344, 0); SB; \
  MM_U(0, 0, 12, 1); SB; RH_U(1, 0, 57344, 1); SB; MM_U(0, 1, 12, 0); SB; RH_U(1, 1, 57344, 0); SB; LDA1(1); SB; MM_U(0, 1, 12, 1); SB; \
  RH_U(1, 1, 57344, 1); SB; MM_U(0, 2, 12, 0); SB; RH_U(1, 2, 57344, 0); SB; MM_U(0, 2, 12, 1); SB; RH_U(1, 2, 57344, 1); SB; LDA0(2); SB; \
  MM_U(0, 3, 12, 0); SB; RH_U(1, 3, 57344, 0); SB; MM_U(0, 3, 12, 1); SB; RH_U(1, 3, 57344, 1); SB; CSUM(csa, 1, 0, 0); SB; LDA1(2); SB; \
  SETADP(0, 65536, 1); SB; SETADP(1, 65536, 1); SB; SETADP(2, 65536, 1); SB; SETADP(3, 65536, 1); SB; CS_OUT(2, csa); WAITL(); WAITV(17); \
  BARRIER_RAW(); SB; \
  MM_U(1, 0, 13, 0); SB; RH_P(0, 0, 32768, 0); SB; MM_U(1, 0, 13, 1); SB; RH_P(0, 0, 32768, 1); SB; MM_U(1, 1, 13, 0); SB; RH_P(0, 1, 32768, 0); SB; \
  POLLR(WS_FR + head * 128); SB; MM_U(1, 1, 13, 1); SB; RH_P(0, 1, 32768, 1); SB; MM_U(1, 2, 13, 0); SB; RH_P(0, 2, 32768, 0); SB; \
  MM_U(1, 2, 13, 1); SB; RH_P(0, 2, 32768, 1); SB; LDA0(3); SB; MM_U(1, 3, 13, 0); SB; RH_P(0, 3, 32768, 0); SB; MM_U(1, 3, 13, 1); SB; \
  RH_P(0, 3, 32768, 1); SB; LDA1(3); SB; SETADP(0, 0, 1); SB; SETADP(1, 0, 1); SB; SETADP(2, 0, 1); SB; SETADP(3, 0, 1); SB; \
  epi_h(1, [&](int k_) __attribute__((always_inline)) { SB; if (k_ == 0) {  } if (k_ == 1) {  } if (k_ == 2) {  } if (k_ == 3) {  } SB; }); WAITV(10); \
  PINA(0); MM_P(0, 0, 0, 0); SB; RH_P(1, 0, 0, 0); SB; MM_P(0, 0, 0, 1); SB; RH_P(1, 0, 0, 1); SB; MM_P(0, 1, 0, 0); SB; RH_P(1, 1, 0, 0); SB; \
  MM_P(0, 1, 0, 1); SB; RH_P(1, 1, 0, 1); SB; MM_P(0, 2, 0, 0); SB; RH_P(1, 2, 0, 0); SB; MM_P(0, 2, 0, 1); SB; RH_P(1, 2, 0, 1); SB; \
  MM_P(0, 3, 0, 0); SB; RH_P(1, 3, 0, 0); SB; MM_P(0, 3, 0, 1); SB; RH_P(1, 3, 0, 1); SB; WAITV(12); phi_q(); CHECKR(WS_FR + head * 128, 4); WAITV(0); \
  BARRIER_RAW(); SB; \
  FLAG_H(1); PINA(1); PINA(2); PINA(3); MM_P(1, 0, 1, 0); SB; RH_P(0, 0, 32768, 0); SB; MM_P(1, 0, 1, 1); SB; RH_P(0, 0, 32768, 1); SB; \
  MM_P(1, 1, 1, 0); SB; RH_P(0, 1, 32768, 0); SB; LDKV(); SB; MM_P(1, 1, 1, 1); SB; RH_P(0, 1, 32768, 1); SB; MM_P(1, 2, 1, 0); SB; \
  RH_P(0, 2, 32768, 0); SB; MM_P(1, 2, 1, 1); SB; RH_P(0, 2, 32768, 1); SB; LDA0(4); SB; MM_P(1, 3, 1, 0); SB; RH_P(0, 3, 32768, 0); SB; \
  MM_P(1, 3, 1, 1); SB; RH_P(0, 3, 32768, 1); SB; LDA1(4); SB; MM_P(0, 0, 2, 0); SB; RH_P(1, 0, 16384, 0); SB; MM_P(0, 0, 2, 1); SB; \
  RH_P(1, 0, 16384, 1); SB; LDA0(5); SB; MM_P(0, 1, 2, 0); SB; RH_P(1, 1, 16384, 0); SB; MM_P(0, 1, 2, 1); SB; RH_P(1, 1, 16384, 1); SB; LDA1(5); SB; \
  MM_P(0, 2, 2, 0); SB; RH_P(1, 2, 16384, 0); SB; MM_P(0, 2, 2, 1); SB; RH_P(1, 2, 16384, 1); SB; ISS0(P, 7, 8); SB; MM_P(0, 3, 2, 0); SB; \
  RH_P(1, 3, 16384, 0); SB; MM_P(0, 3, 2, 1); SB; RH_P(1, 3, 16384, 1); SB; ISS1(P, 7, 8); SB; SETADP(0, 65536, 1); SB; SETADP(1, 65536, 1); SB; \
  SETADP(2, 65536, 1); SB; SETADP(3, 65536, 1); SB; MM_P(1, 0, 3, 0); SB; RH_P(0, 0, 0, 0); SB; MM_P(1, 0, 3, 1); SB; RH_P(0, 0, 0, 1); SB; \
  ISS0(P, 8, 7); SB; MM_P(1, 1, 3, 0); SB; RH_P(0, 1, 0, 0); SB; MM_P(1, 1, 3, 1); SB; RH_P(0, 1, 0, 1); SB; ISS1(P, 8, 7); SB; MM_P(1, 2, 3, 0); SB; \
  RH_P(0, 2, 0, 0); SB; MM_P(1, 2, 3, 1); SB; RH_P(0, 2, 0, 1); SB; ISS0(P, 9, 6); SB; MM_P(1, 3, 3, 0); SB; RH_P(0, 3, 0, 0); SB; \
  MM_P(1, 3, 3, 1); SB; RH_P(0, 3, 0, 1); SB; ISS1(P, 9, 6); SB; SETADP(0, 0, 1); SB; SETADP(1, 0, 1); SB; SETADP(2, 0, 1); SB; SETADP(3, 0, 1); SB; \
  KVF_SHARE(10); WAITL(); BARRIER_RAW(); SB; \
  o_compute(0, [&](int k_) __attribute__((always_inline)) { SB; if (k_ == 0) { POLL(WS_FH + (rg * 2 + 1) * 128); ISS0(P, 10, 0); } if (k_ == 1) { ISS1(P, 10, 0); ISS0(P, 11, 2); } if (k_ == 2) { ISS1(P, 11, 2); ISS0(P, 12, 1); } if (k_ == 3) { ISS1(P, 12, 1); ISS0(P, 13, 4); ISS1(P, 13, 4); } SB; }); \
  CHECK(WS_FH + (rg * 2 + 1) * 128, 0xc7c7, 8); WAITV(14); BARRIER_RAW(); SB; \
  PINA(4); PINA(5); MM_P(0, 0, 4, 0); SB; RH_P(1, 0, 49152, 0); SB; MM_P(0, 0, 4, 1); SB; RH_P(1, 0, 49152, 1); SB; \
  POLL(WS_FH + (rg * 2 + 1) * 128); SB; MM_P(0, 1, 4, 0); SB; RH_P(1, 1, 49152, 0); SB; MM_P(0, 1, 4, 1); SB; RH_P(1, 1, 49152, 1); SB; LDA0(6); SB; \
  MM_P(0, 2, 4, 0); SB; RH_P(1, 2, 49152, 0); SB; MM_P(0, 2, 4, 1); SB; RH_P(1, 2, 49152, 1); SB; LDA1(6); SB; MM_P(0, 3, 4, 0); SB; \
  RH_P(1, 3, 49152, 0); SB; MM_P(0, 3, 4, 1); SB; RH_P(1, 3, 49152, 1); SB; LDA0(7); SB; SETADP(0, 65536, 1); SB; SETADP(1, 65536, 1); SB; \
  SETADP(2, 65536, 1); SB; SETADP(3, 65536, 1); SB; MM_P(1, 0, 5, 0); SB; RH_P(0, 0, 16384, 0); SB; MM_P(1, 0, 5, 1); SB; RH_P(0, 0, 16384, 1); SB; \
  LDA1(7); SB; MM_P(1, 1, 5, 0); SB; RH_P(0, 1, 16384, 0); SB; MM_P(1, 1, 5, 1); SB; RH_P(0, 1, 16384, 1); SB; LDA0(8); SB; MM_P(1, 2, 5, 0); SB; \
  RH_P(0, 2, 16384, 0); SB; LDA1(8); SB; MM_P(1, 2, 5, 1); SB; RH_P(0, 2, 16384, 1); SB; MM_P(1, 3, 5, 0); SB; RH_P(0, 3, 16384, 0); SB; LDA0(9); SB; \
  MM_P(1, 3, 5, 1); SB; RH_P(0, 3, 16384, 1); SB; LDA1(9); SB; SETADP(0, 131072, 1); SB; SETADP(1, 131072, 1); SB; SETADP(2, 131072, 1); SB; \
  SETADP(3, 131072, 1); SB; CHECK(WS_FH + (rg * 2 + 1) * 128, 0xffff, 8); WAITV(0); BARRIER_RAW(); SB; \
  FLAG_O(); LEAVE(); PINA(6); PINA(7); MM_P(0, 0, 6, 0); SB; RH_P(1, 0, 0, 0); SB; MM_P(0, 0, 6, 1); SB; RH_P(1, 0, 0, 1); SB; LDA0(10); SB; \
  MM_P(0, 1, 6, 0); SB; RH_P(1, 1, 0, 0); SB; MM_P(0, 1, 6, 1); SB; RH_P(1, 1, 0, 1); SB; LDA1(10); SB; MM_P(0, 2, 6, 0); SB; RH_P(1, 2, 0, 0); SB; \
  MM_P(0, 2, 6, 1); SB; RH_P(1, 2, 0, 1); SB; LDA0(11); SB; MM_P(0, 3, 6, 0); SB; RH_P(1, 3, 0, 0); SB; MM_P(0, 3, 6, 1); SB; RH_P(1, 3, 0, 1); SB; \
  LDA1(11); SB; SETADP(0, 65536, 1); SB; SETADP(1, 65536, 1); SB; SETADP(2, 65536, 1); SB; SETADP(3, 65536, 1); SB; MM_P(1, 0, 7, 0); SB; \
  RH_P(0, 0, 49152, 0); SB; MM_P(1, 0, 7, 1); SB; RH_P(0, 0, 49152, 1); SB; LDA0(12); SB; MM_P(1, 1, 7, 0); SB; RH_P(0, 1, 49152, 0); SB; \
  MM_P(1, 1, 7, 1); SB; RH_P(0, 1, 49152, 1); SB; LDA1(12); SB; MM_P(1, 2, 7, 0); SB; RH_P(0, 2, 49152, 0); SB; MM_P(1, 2, 7, 1); SB; \
  RH_P(0, 2, 49152, 1); SB; ISS0(P, 14, 3); SB; MM_P(1, 3, 7, 0); SB; RH_P(0, 3, 49152, 0); SB; MM_P(1, 3, 7, 1); SB; RH_P(0, 3, 49152, 1); SB; \
  ISS1(P, 14, 3); SB; WAITV(8); BARRIER_RAW(); SB; \
  PINA(8); PINA(9); MM_P(0, 0, 8, 0); SB; RH_P(1, 0, 32768, 0); SB; MM_P(0, 0, 8, 1); SB; RH_P(1, 0, 32768, 1); SB; MM_P(0, 1, 8, 0); SB; \
  RH_P(1, 1, 32768, 0); SB; POLL(WS_FO + rg * 128); SB; MM_P(0, 1, 8, 1); SB; RH_P(1, 1, 32768, 1); SB; MM_P(0, 2, 8, 0); SB; RH_P(1, 2, 32768, 0); SB; \
  MM_P(0, 2, 8, 1); SB; RH_P(1, 2, 32768, 1); SB; LDA0(13); SB; MM_P(0, 3, 8, 0); SB; RH_P(1, 3, 32768, 0); SB; MM_P(0, 3, 8, 1); SB; \
  RH_P(1, 3, 32768, 1); SB; LDA1(13); SB; SETADP(0, 0, 1); SB; SETADP(1, 0, 1); SB; SETADP(2, 0, 1); SB; SETADP(3, 0, 1); SB; MM_P(1, 0, 9, 0); SB; \
  RH_P(0, 0, 0, 0); SB; MM_P(1, 0, 9, 1); SB; RH_P(0, 0, 0, 1); SB; MM_P(1, 1, 9, 0); SB; RH_P(0, 1, 0, 0); SB; XRES(); SB; MM_P(1, 1, 9, 1); SB; \
  RH_P(0, 1, 0, 1); SB; MM_P(1, 2, 9, 0); SB; RH_P(0, 2, 0, 0); SB; MM_P(1, 2, 9, 1); SB; RH_P(0, 2, 0, 1); SB; ISS0(P, 15, 5); SB; \
  MM_P(1, 3, 9, 0); SB; RH_P(0, 3, 0, 0); SB; MM_P(1, 3, 9, 1); SB; RH_P(0, 3, 0, 1); SB; ISS1(P, 15, 5); SB; CHECK(WS_FO + rg * 128, 0xffff, 5); \
  WAITV(9); BARRIER_RAW(); SB; \
  PINA(10); PINA(11); MM_P(0, 0, 10, 0); SB; RH_P(1, 0, 32768, 0); SB; MM_P(0, 0, 10, 1); SB; RH_P(1, 0, 32768, 1); SB; LDA0(14); SB; \
  MM_P(0, 1, 10, 0); SB; RH_P(1, 1, 32768, 0); SB; MM_P(0, 1, 10, 1); SB; RH_P(1, 1, 32768, 1); SB; LDA1(14); SB; MM_P(0, 2, 10, 0); SB; \
  RH_P(1, 2, 32768, 0); SB; MM_P(0, 2, 10, 1); SB; RH_P(1, 2, 32768, 1); SB; LDA0(15); SB; MM_P(0, 3, 10, 0); SB; RH_P(1, 3, 32768, 0); SB; \
  MM_P(0, 3, 10, 1); SB; RH_P(1, 3, 32768, 1); SB; LDA1(15); SB; MM_P(1, 0, 11, 0); SB; RH_P(0, 0, 16384, 0); SB; MM_P(1, 0, 11, 1); SB; \
  RH_P(0, 0, 16384, 1); SB; LDA0(16); SB; MM_P(1, 1, 11, 0); SB; RH_P(0, 1, 16384, 0); SB; MM_P(1, 1, 11, 1); SB; RH_P(0, 1, 16384, 1); SB; \
  LDA1(16); SB; MM_P(1, 2, 11, 0); SB; RH_P(0, 2, 16384, 0); SB; MM_P(1, 2, 11, 1); SB; RH_P(0, 2, 16384, 1); SB; ISS0(P, 16, 8); SB; \
  MM_P(1, 3, 11, 0); SB; RH_P(0, 3, 16384, 0); SB; MM_P(1, 3, 11, 1); SB; RH_P(0, 3, 16384, 1); SB; ISS1(P, 16, 8); SB; SETADP(0, 65536, 1); SB; \
  SETADP(1, 65536, 1); SB; SETADP(2, 65536, 1); SB; SETADP(3, 65536, 1); SB; WAITV(11); BARRIER_RAW(); SB; \
  PINA(12); PINA(13); MM_P(0, 0, 12, 0); SB; RH_P(1, 0, 0, 0); SB; MM_P(0, 0, 12, 1); SB; RH_P(1, 0, 0, 1); SB; MM_P(0, 1, 12, 0); SB; \
  RH_P(1, 1, 0, 0); SB; MM_P(0, 1, 12, 1); SB; RH_P(1, 1, 0, 1); SB; LDA0(17); SB; MM_P(0, 2, 12, 0); SB; RH_P(1, 2, 0, 0); SB; MM_P(0, 2, 12, 1); SB; \
  RH_P(1, 2, 0, 1); SB; MM_P(0, 3, 12, 0); SB; RH_P(1, 3, 0, 0); SB; MM_P(0, 3, 12, 1); SB; RH_P(1, 3, 0, 1); SB; LDA1(17); SB; SETADP(0, 0, 0); SB; \
  SETADP(1, 0, 0); SB; SETADP(2, 0, 0); SB; SETADP(3, 0, 0); SB; MM_P(1, 0, 13, 0); SB; RH_P(0, 0, 49152, 0); SB; MM_P(1, 0, 13, 1); SB; \
  RH_P(0, 0, 49152, 1); SB; MM_P(1, 1, 13, 0); SB; RH_P(0, 1, 49152, 0); SB; MM_P(1, 1, 13, 1); SB; RH_P(0, 1, 49152, 1); SB; ISS0(P, 17, 7); SB; \
  MM_P(1, 2, 13, 0); SB; RH_P(0, 2, 49152, 0); SB; MM_P(1, 2, 13, 1); SB; RH_P(0, 2, 49152, 1); SB; MM_P(1, 3, 13, 0); SB; RH_P(0, 3, 49152, 0); SB; \
  MM_P(1, 3, 13, 1); SB; RH_P(0, 3, 49152, 1); SB; ISS1(P, 17, 7); SB; SETADP(0, 65536, 0); SB; SETADP(1, 65536, 0); SB; SETADP(2, 65536, 0); SB; \
  SETADP(3, 65536, 0); SB; WAITV(4); BARRIER_RAW(); SB; \
  PINA(14); PINA(15); MM_P(0, 0, 14, 0); SB; RH_P(1, 0, 16384, 0); SB; MM_P(0, 0, 14, 1); SB; RH_P(1, 0, 16384, 1); SB; MM_P(0, 1, 14, 0); SB; \
  RH_P(1, 1, 16384, 0); SB; MM_P(0, 1, 14, 1); SB; RH_P(1, 1, 16384, 1); SB; MM_P(0, 2, 14, 0); SB; RH_P(1, 2, 16384, 0); SB; MM_P(0, 2, 14, 1); SB; \
  RH_P(1, 2, 16384, 1); SB; MM_P(0, 3, 14, 0); SB; RH_P(1, 3, 16384, 0); SB; MM_P(0, 3, 14, 1); SB; RH_P(1, 3, 16384, 1); SB; SETADP(0, 131072, 0); SB; \
  SETADP(1, 131072, 0); SB; SETADP(2, 131072, 0); SB; SETADP(3, 131072, 0); SB; MM_P(1, 0, 15, 0); SB; RH_P(0, 0, 0, 0); SB; MM_P(1, 0, 15, 1); SB; \
  RH_P(0, 0, 0, 1); SB; MM_P(1, 1, 15, 0); SB; RH_P(0, 1, 0, 0); SB; MM_P(1, 1, 15, 1); SB; RH_P(0, 1, 0, 1); SB; MM_P(1, 2, 15, 0); SB; \
  RH_P(0, 2, 0, 0); SB; MM_P(1, 2, 15, 1); SB; RH_P(0, 2, 0, 1); SB; MM_P(1, 3, 15, 0); SB; RH_P(0, 3, 0, 0); SB; MM_P(1, 3, 15, 1); SB; \
  RH_P(0, 3, 0, 1); SB; SETADP(0, 65536, 0); SB; SETADP(1, 65536, 0); SB; SETADP(2, 65536, 0); SB; SETADP(3, 65536, 0); SB; WAITV(0); \
  BARRIER_RAW(); SB; \
  PINA(16); MM_P(0, 0, 16, 0); SB; RH_P(1, 0, 49152, 0); SB; MM_P(0, 0, 16, 1); SB; RH_P(1, 0, 49152, 1); SB; MM_P(0, 1, 16, 0); SB; \
  RH_P(1, 1, 49152, 0); SB; MM_P(0, 1, 16, 1); SB; RH_P(1, 1, 49152, 1); SB; MM_P(0, 2, 16, 0); SB; RH_P(1, 2, 49152, 0); SB; MM_P(0, 2, 16, 1); SB; \
  RH_P(1, 2, 49152, 1); SB; MM_P(0, 3, 16, 0); SB; RH_P(1, 3, 49152, 0); SB; MM_P(0, 3, 16, 1); SB; RH_P(1, 3, 49152, 1); SB; FINAL_PREP(); SB; \
  WAITV(0); PINA(17); MM_P(1, 0, 17, 0); SB; MM_P(1, 0, 17, 1); SB; MM_P(1, 1, 17, 0); SB; MM_P(1, 1, 17, 1); SB; MM_P(1, 2, 17, 0); SB; \
  MM_P(1, 2, 17, 1); SB; MM_P(1, 3, 17, 0); SB; MM_P(1, 3, 17, 1); SB; FINAL();

__global__ __launch_bounds__(512, 2) void fused_kernel(const char* x, const char* wqkv, const char* wphi, const char* wo, const char* wg, const char* wu,
    const char* wd, char* out, char* ws) {
  LAS char* lds = (LAS char*)(u32)0;
  const int tid = threadIdx.x;
  u32 lane = tid & 63;
  u32 w = rfl(tid >> 6), b = blockIdx.x;
  u32 m, n, idx, ch, rgl, rg, jj, qm, prod, head, u0, u1, wb1, wb2, urs, ustr, sofR, soo, aso, kw, pw0, pw1, pw2;
  bool isq;
  u64 epoch;
  srd_t sX, sQ, sQb, sP, sW, sG, sU, sD, sO, sU0a, sU0b;
  u32 c16, g, rho, cc, l16, tok, csad, stad, bU, bK, bA, bB, bV, bP, bO, wra, wrb, kra, krb;
  u32 dso, vx0, vx1, vU, vV, vk, lcU, vD[4], xr[4], ad[8], vo, vA, vAo;
  const f32x4 zero4 = {0.f, 0.f, 0.f, 0.f};
  u32x4 xa[14], xb[14], A[18][2], xres, ones, qf;
  u32x2 pv, kvp, part[16];
  bf16x8 F[2][4], fa[4], fb[4];
  f32x4 acc[2][4], ack[2][2], ap[2][4], csk, csa, s1a, s2a;
  float nma, rsa, sd2, nmb, rsb;

  auto pre0 = [&]() __attribute__((always_inline)) {
    idx = b >> 3; rgl = idx >> 3; rg = (b & 6) * 2 + rgl; jj = idx & 7; wb2 = w * 2048;
    kw = (u32)(0x1065432106543210ull >> (4 * ((u32)(0x1065432106543210ull >> (4 * (ROT))) & 15)));
    rho = lane >> 3; cc = lane & 7;
    vx0 = (rg * 128 + 16 * w + rho) * XS + 16 * (cc ^ (rho >> 1)); vx1 = (vx0 ^ 64) + (8 * XS - 1024);
    ch = b & 1;
    isq = jj >= 6; qm = isq ? ~0u : 0u; urs = isq ? QS : GS; ustr = 64 * urs;
    u0 = 14 * ch + 2 * jj;
    lcU = (cc ^ ((rho & 2) | ((w & 1) << 2))) * 16;
    vU = (8 * w + rho) * urs + ((224 * (2 * ch + (jj & 1)) & qm) | (128 * u0 & ~qm)) + lcU - ((lcU >> 2) & 16 & qm);
    asm volatile("" : "+v"(vx0), "+v"(vx1), "+v"(vU));
    asm volatile("" : "+s"(kw), "+s"(wb2), "+s"(ustr));
    asm volatile("" : "+s"(x), "+s"(wqkv), "+s"(wphi), "+s"(wo), "+s"(wg), "+s"(wu), "+s"(wd), "+s"(out), "+s"(ws));
    sX = mk_srd(x, S_ * XS);
  };
  auto pre1 = [&]() __attribute__((always_inline)) {
    asm volatile("" : "+s"(wqkv), "+s"(wg), "+s"(wu));
    sU0a = mk_srd(isq ? wqkv : wg, ustr * 7); sU0b = mk_srd(isq ? wqkv - 912 : wu - 1024, ustr * 7 + (isq ? 912 : 1024));
  };
  auto traddr = [&](u32 o, u32 x) __attribute__((always_inline)) { return ((bU & ~31u) ^ x) ^ ((bU & 24) + o); };
  auto setup0 = [&]() __attribute__((always_inline)) {
    asm volatile("" : "+v"(lane));
    asm volatile("" : "+v"(rho), "+v"(lcU));
    asm volatile("" : "+s"(w), "+s"(jj), "+s"(wqkv));
    m = w & 3; n = w >> 2; wb1 = w * 1024; prod = 8 * ch + jj;
    head = (u32)(0xFEDB9CA875642310ull >> (4 * prod)) & 15;
    sQ = mk_srd(wqkv, D_ * QS); sQb = mk_srd(wqkv - 1024, D_ * QS + 1024);
    vk = (8 * w + rho) * QS + (896 + ((56 * head) & ~15u)) + lcU + (lcU >> 6) * 832;
    l16 = lane * 16; c16 = lane & 15; g = lane >> 4;
    tok = 32 * m + 16 * n + c16;
    xr[0] = tok * 128 + 16 * (g ^ (c16 >> 1)); xr[1] = xr[0] ^ 64; xr[2] = xr[0] ^ 2048; xr[3] = xr[1] ^ 2048;
    bO = ((lane & 12) << 5) | ((lane & 24) << 2) | ((lane & 3) << 3);
    bU = bO | (g << 11);
    bK = traddr((head & 1) * 8, (n << 6) | ((m & 1) << 5));
    ones = u32x4{0x3f803f80u, 0x3f803f80u, 0x3f803f80u, 0x3f803f80u};
    asm volatile("" : "+v"(ones));
  };
  auto setup1 = [&]() __attribute__((always_inline)) {
    asm volatile("" : "+v"(bU));
    asm volatile("s_waitcnt lgkmcnt(0)" : "+s"(epoch));
    epoch += 0x9E3779B97F4A7C15ull;
    const u32 h = m >> 1, xv = (n << 6) | ((m & 1) << 5) | (h << 10), sw = 0x440 & qm;
    bV = bU ^ xv;
    bA = traddr(8 * h & qm, xv ^ ((n ^ h) ? sw : 0u)); bB = traddr(8 * (h ^ 1) & qm, xv ^ 1024 ^ ((n ^ h) ? 0u : sw));
    asm volatile("" : "+v"(bV));
  };
  auto setup2 = [&]() __attribute__((always_inline)) {
    asm volatile("" : "+v"(rho), "+v"(lcU), "+v"(tok), "+v"(g));
    asm volatile("" : "+s"(wphi), "+s"(wg), "+s"(wu), "+s"(ws), "+s"(u0));
    sP = mk_srd(wphi + head * 1568, 1568); sW = mk_srd(ws, WS_END); sG = mk_srd(wg, D_ * GS); sU = mk_srd(wu - 1024, D_ * GS + 1024);
    u1 = u0 + (jj == 7 ? -1 : jj == 6 ? 0 : 1);
    vV = (8 * w + rho) * GS + lcU + 128 * u1;
    csad = SC_CS + w * 64 + g * 16; stad = SC_ST0 + tok * 16;
    *(LAS u32x2*)(lds + SC_V + tok * 64 + ((~tok & 8) << 2) + 24) = u32x2{0x00003f80u, 0u};
    asm volatile("" : "+v"(vV), "+v"(csad), "+v"(stad));
  };
  auto setup3 = [&]() __attribute__((always_inline)) {
    asm volatile("" : "+v"(rho), "+v"(cc));
    asm volatile("" : "+s"(wd), "+s"(wo), "+s"(rg));
    sD = mk_srd(wd, F_ * XS); sO = mk_srd(wo, D_ * XS);
    sofR = WS_PART + head * 131072 + rg * 512;
    vAo = l16 + WS_HB + ((rg * 4 + 2 * ch + n) * 72 + 56 + m) * 2048; vA = vAo + ((m >> 1) * 26 - 56) * 2048; soo = (rg * 128 + 64 * ch) * XS + 128 * jj;
    vD[0] = rho * XS + 16 * (cc ^ (rho & 6)) + 128 * jj + (896 * (w >> 2) + 16 * (w & 3)) * XS; vD[1] = vD[0] + (8 * XS - 1024);
    const u32 r = 4 * (jj + rgl);
    pw0 = (u32)(0x0420420420420420ull >> r) & 0xFFF; pw0 |= (pw0 + 0x666) << 12; pw1 = (u32)(0x1DC531DC531DC531ull >> r);
    pw2 = ((u32)(0x7B97B97B97B97B97ull >> r) & 0xFFF) | ((u32)(0x3210321032103210ull >> r) << 16);
    asm volatile("" : "+v"(vD[0]), "+v"(vD[1]));
    asm volatile("" : "+s"(pw0), "+s"(pw1), "+s"(pw2));
    vD[2] = vD[0] + (28 * (w >> 1) + 16 * (w & 1) - 896 * (w >> 2) - 16 * (w & 3)) * XS;
    vD[3] = ((w & 1) && rho >= 4) ? 0x80000000u : vD[2] + (8 * XS - 1024);
    vo = (8 * w + rho) * XS + 16 * cc;
  };
  auto setup4 = [&]() __attribute__((always_inline)) {
    asm volatile("" : "+v"(bO));
    wra = SC_WP + (lane >> 2) * 56 + (bO & 24); wrb = wra + ((bO & 24) == 24 ? 24 : 32);
    const u32 b1 = bO | (g << 10);
    kra = b1 ^ ((w >> 1) << 5); krb = SC_V + (((b1 >> 1) & ~63u) | (b1 & 24) | (((b1 >> 1) ^ (w << 5)) & 32));
    bO = bO | (g << 9) | (m << 12); bP = bO ^ (n << 11);
    asm volatile("" : "+v"(wra), "+v"(wrb), "+v"(kra), "+v"(krb), "+v"(bO), "+v"(bP));
  };
  auto stats_out = [&]() __attribute__((always_inline)) {
    float s2, hi;
    asm volatile("s_nop 7\n\tv_cndmask_b32 %0, %2, %3, %6\n\tv_cndmask_b32 %1, %4, %5, %6\n\tv_cndmask_b32 %0, %0, %1, %7"
        : "=&v"(s2), "=&v"(hi) : "v"(s2a[0]), "v"(s2a[1]), "v"(s2a[2]), "v"(s2a[3]), "s"(0xAAAAAAAAAAAAAAAAull), "s"(0xCCCCCCCCCCCCCCCCull));
    asm volatile("" : "+v"(s1a));
    const float mean = s1a[0] * (1.f / 448.f);
    const float pp = s1a[0] * s1a[0], pe = __builtin_fmaf(s1a[0], s1a[0], -pp);
    const float var = relu((__builtin_fmaf(448.f, s2, -pp) - pe) * (1.f / (448.f * 448.f)));
    const float sd = rbf(__builtin_amdgcn_sqrtf(rbf(rbf(var) + 1e-5f))), rs = __builtin_amdgcn_rcpf(sd);
    u32 a;
    asm("v_cndmask_b32 %0, %1, %2, %3" : "=v"(a) : "v"(SC_DUMP), "v"(stad), "s"(0xF0000F0000F0000Full));
    *(LAS f32x4*)(lds + a) = f32x4{rbf(-mean), rs, sd * sd, rs * rs};
  };
  auto pin_acc = [&]() __attribute__((always_inline)) {
    asm volatile("s_nop 7" : "+v"(acc[0][0]), "+v"(acc[0][1]), "+v"(acc[0][2]), "+v"(acc[0][3]), "+v"(acc[1][0]), "+v"(acc[1][1]), "+v"(acc[1][2]), "+v"(acc[1][3]));
  };
  auto wphi_frags = [&](bf16x8& w0, bf16x8& w1) __attribute__((always_inline)) {
    const u32 h0 = (m & 1) * 896, h1 = 896 - h0;
    w0 = trfrag(lds + (wra + h0), lds + (wra + h1)); w1 = trfrag(lds + (wrb + h0), lds + (wrb + h1));
  };
  auto epi_h = [&](int u, auto hook) __attribute__((always_inline)) {
    const f32x4 c[4] = {CS_IN(2 * u, 0), CS_IN(2 * u, 1), CS_IN(2 * u, 2), CS_IN(2 * u, 3)};
    SB;
    hook(0);
    pin_acc();
#pragma unroll
    for (int i = 0; i < 4; ++i) { acc[0][i] = fix4(acc[0][i], c[i], nma); acc[1][i] = fix4(acc[1][i], c[i], nmb); }
    if (u) asm volatile("" : "+v"(acc[0][0]), "+v"(acc[0][1]), "+v"(acc[0][2]), "+v"(acc[0][3]), "+v"(acc[1][0]), "+v"(acc[1][1]), "+v"(acc[1][2]), "+v"(acc[1][3]));
    hook(1);
    if (u == 0 && isq) {
#pragma unroll
      for (int i = 0; i < 4; ++i) {
        const int tt = i & 1, hh = i >> 1;
        const float rs = (tt ? rsb : rsa) * LOG2E;
        st128(pack8(acc[tt][2 * hh] * rs, acc[tt][2 * hh + 1] * rs), sW,
              l16 + WS_QB + ((rg * 8 + 2 * m + (n ^ tt)) * 16 + 8 * ch + 4 * (jj & 1) + 2 * n + (hh ^ (m >> 1))) * 1024);
        SB;
      }
    } else {
#define RELU1(a) "v_max_f32 %" #a ", 0, %" #a "\n\t"
#pragma unroll
      for (int i = 0; i < 2; ++i) {
        f32x4 &p = acc[i][0], &q = acc[i][1], &r = acc[i][2], &s = acc[i][3];
        asm volatile("s_cmp_lg_u32 %16, 0\n\ts_cbranch_scc1 1f\n\t" RELU1(0) RELU1(1) RELU1(2) RELU1(3) RELU1(4) RELU1(5) RELU1(6) RELU1(7) "s_branch 2f\n1:\n\t"
            RELU1(8) RELU1(9) RELU1(10) RELU1(11) RELU1(12) RELU1(13) RELU1(14) RELU1(15) "\n2:"
            : "+v"(p[0]), "+v"(p[1]), "+v"(p[2]), "+v"(p[3]), "+v"(q[0]), "+v"(q[1]), "+v"(q[2]), "+v"(q[3]),
              "+v"(r[0]), "+v"(r[1]), "+v"(r[2]), "+v"(r[3]), "+v"(s[0]), "+v"(s[1]), "+v"(s[2]), "+v"(s[3]) : "s"(m & 2) : "scc");
        hook(2 + i);
      }
#pragma unroll
      for (int tt = 0; tt < 2; ++tt)
        st128(pack8(acc[tt][0] * acc[tt][2], acc[tt][1] * acc[tt][3]), sW, l16 + WS_HB + (((rg * 4 + m) * 72 + 2 * (u ? u1 : u0) + n) * 2 + (n ^ tt)) * 1024);
    }
    SB;
  };
  auto kv_xchg = [&](int s) __attribute__((always_inline)) {
    asm volatile("" : "+v"(ack[0][0]), "+v"(ack[0][1]), "+v"(ack[1][0]), "+v"(ack[1][1]));
    LAS char* p = lds + (s * SLOT + (2 * m + (n ^ 1)) * 2048 + l16);
    *(LAS f32x4*)p = ack[1][0]; *(LAS f32x4*)(p + 1024) = ack[1][1];
  };
  bf16x8 wf0, wf1; u32 va0, va1, pka;
  auto kv_prep = [&](int s) __attribute__((always_inline)) {
    wphi_frags(wf0, wf1);
    const u32 v0 = SC_V + tok * 64 + 8 * g + ((tok & 8) << 2), v1 = g == 3 ? SC_DUMP : (v0 ^ 32);
    va0 = (m & 1) ? v1 : v0; va1 = (m & 1) ? v0 : v1;
    pka = s * SLOT + tok * 128 + 8 * g + (((tok & 2) | ((tok & 8) >> 1)) << 4);
    asm volatile("" : "+v"(va0), "+v"(va1), "+v"(pka));
  };
  auto kv_feat = [&](int s, auto hook) __attribute__((always_inline)) {
    LAS char* px = lds + (s * SLOT + (2 * m + n) * 2048 + l16);
    const u32 ck = csad & ~256u, cv = csad | 256u;
    f32x4 r0 = *(LAS f32x4*)px, r1 = *(LAS f32x4*)(px + 1024);
    const f32x4 c0 = *(LAS f32x4*)(lds + ck + 512), c1 = *(LAS f32x4*)(lds + (ck ^ 64) + 512);
    const f32x4 c4 = *(LAS f32x4*)(lds + cv + 512), c5 = *(LAS f32x4*)(lds + (cv ^ 64) + 512);
    hook(0);
    asm volatile("s_cmp_eq_u32 %16, 0\n\ts_cbranch_scc1 1f\n\tv_swap_b32 %0, %8\n\tv_swap_b32 %1, %9\n\tv_swap_b32 %2, %10\n\tv_swap_b32 %3, %11\n\t"
        "v_swap_b32 %4, %12\n\tv_swap_b32 %5, %13\n\tv_swap_b32 %6, %14\n\tv_swap_b32 %7, %15\n1:"
        : "+v"(ack[0][0][0]), "+v"(ack[0][0][1]), "+v"(ack[0][0][2]), "+v"(ack[0][0][3]), "+v"(ack[0][1][0]), "+v"(ack[0][1][1]), "+v"(ack[0][1][2]), "+v"(ack[0][1][3]),
          "+v"(r0[0]), "+v"(r0[1]), "+v"(r0[2]), "+v"(r0[3]), "+v"(r1[0]), "+v"(r1[1]), "+v"(r1[2]), "+v"(r1[3]) : "s"(n) : "scc");
    const float rl = rsa * LOG2E;
    const bf16x8 kf = as_frag(pack8(fix4(ack[0][0], c0, nma) * rl, fix4(ack[0][1], c1, nma) * rl));
    f32x4 p0 = mfma(wf0, kf, zero4), p1 = mfma(wf1, kf, zero4);
    asm volatile("" : "+v"(p0), "+v"(p1), "+v"(r0), "+v"(r1));
    hook(1);
    *(LAS u32x2*)(lds + va0) = pack4(fix4(r0, c4, nma) * rsa); *(LAS u32x2*)(lds + va1) = pack4(fix4(r1, c5, nma) * rsa);
    hook(2);
    hook(3);
    f32x4 ep0, ep1, em0, em1;
    dual_exp(p0, p1, ep0, ep1, em0, em1, hook, 4);
    const float w1 = g == 3 ? 0.f : 1.f;
    const f32x4 sp4 = fix4(ep0, ep1, w1), sm4 = fix4(em0, em1, w1);
    float sp = (sp4[0] + sp4[1]) + (sp4[2] + sp4[3]), sm = (sm4[0] + sm4[1]) + (sm4[2] + sm4[3]);
    asm volatile("" : "+v"(sp), "+v"(sm));
    hook(7);
    asm volatile(PAIR("v_add_f32", "v_rcp_f32 %0, %0\n\ts_nop 0\n\t") : "+v"(sp), "+v"(sm));
    hook(8);
    const u32 pk = pka;
    *(LAS u32x2*)(lds + pk) = pack4(ep0 * sp); *(LAS u32x2*)(lds + (pk ^ 32)) = pack4(ep1 * (sp * w1));
    hook(9);
    *(LAS u32x2*)(lds + (pk ^ 64)) = pack4(em0 * sm); *(LAS u32x2*)(lds + (pk ^ 96)) = pack4(em1 * (sm * w1));
    SB;
  };
  auto red_store = [&]() __attribute__((always_inline)) {
    asm volatile("; PINA %0 %1 %2 %3 %4 %5 %6 %7 %8 %9 %10 %11 %12 %13 %14 %15" : "+v"(part[0]), "+v"(part[1]), "+v"(part[2]), "+v"(part[3]),
        "+v"(part[4]), "+v"(part[5]), "+v"(part[6]), "+v"(part[7]), "+v"(part[8]), "+v"(part[9]), "+v"(part[10]), "+v"(part[11]),
        "+v"(part[12]), "+v"(part[13]), "+v"(part[14]), "+v"(part[15]));
    f32x2 s[16];
#pragma unroll
    for (int i = 0; i < 16; ++i) s[i] = __builtin_bit_cast(f32x2, part[i]);
#pragma unroll
    for (int d = 8; d; d >>= 1)
#pragma unroll
      for (int i = 0; i < d; ++i) s[i] += s[i + d];
    const f32x2 a = s[0];
    u32 v = pk2(a[0], a[1]);
    if ((rg & 2) && ((lane >> 1) & 15) == 13) v = ((rg & 5) == 5 && lane >= 32) ? 0u : 0x3f803f80u;
    __builtin_amdgcn_raw_buffer_store_b32(v, sW,
        (lane >> 1) * 16 + (lane & 1) * 4 + WS_KVF + head * 4096 + ((rg >> 3) * 2 + ((rg >> 1) & 1)) * 1024 + (rg & 1) * 512 + (rg & 4) * 2, 0, 16);
  };
  u32x4 fp, fm;
  auto phi_q = [&]() __attribute__((always_inline)) {
    wphi_frags(wf0, wf1);
    asm volatile("; PINA %0" : "+v"(qf));
    f32x4 ep0, ep1, em0, em1;
    dual_exp(mfma(wf0, as_frag(qf), zero4), mfma(wf1, as_frag(qf), zero4), ep0, ep1, em0, em1, [](int) {}, 0);
    fp = pack8(ep0, ep1); fm = pack8(em0, em1);
    asm volatile("" : "+v"(fp), "+v"(fm));
    SB;
  };
  auto o_compute = [&](int, auto hook) __attribute__((always_inline)) {
    LAS char* kp = lds + (SC_V + l16);
    const bf16x8 k01 = *(LAS bf16x8*)(kp + 1024), k11 = *(LAS bf16x8*)(kp + 3072), k00 = *(LAS bf16x8*)(kp), k10 = *(LAS bf16x8*)(kp + 2048);
    hook(0);
    const f32x4 p1 = mfma(k01, as_frag(fp), zero4), m1 = mfma(k11, as_frag(fm), zero4), p0 = mfma(k00, as_frag(fp), zero4), m0 = mfma(k10, as_frag(fm), zero4);
    hook(1);
    const float ip = __builtin_amdgcn_rcpf(p1[1]), im = __builtin_amdgcn_rcpf(m1[1]);
    const float c = sd2 * __builtin_amdgcn_rcpf(__builtin_fmaf(p1[0], ip, __builtin_fmaf(m1[0], im, 1e-6f)));
    float a = ip * c, bb = im * c;
    asm volatile("" : "+v"(a), "+v"(bb));
    hook(2);
    auto r_ = __builtin_amdgcn_permlane16_swap(fu(a), fu(bb), false, false);
    auto s_ = __builtin_amdgcn_permlane32_swap(r_[1], r_[1], false, false);
    auto t_ = __builtin_amdgcn_permlane16_swap(s_[1], s_[1], false, false);
    a = uf(t_[0]); bb = uf(t_[1]);
    asm volatile("" : "+v"(a), "+v"(bb));
    hook(3);
    st128(pack8(fix4(p0 * a, m0, bb), fix4(p1 * a, m1, bb)), sW, l16 + WS_HB + (((rg * 4 + m) * 72 + 56 + head) * 2 + n) * 1024);
    SB;
  };
  u32 fw, fr; float r2;
  auto final_prep = [&](u32 fin) __attribute__((always_inline)) {
    fw = fin + m * SLOT + (32 * n + c16) * 256 + ((g ^ (c16 & 7)) << 4);
    fr = fin + (8 * w + rho) * 256 + (((2 * cc) ^ rho) << 4);
    r2 = *(LAS float*)(lds + (SC_ST0 + 12 + (64 * ch + 8 * w + rho) * 16));
    vo += soo;
    asm volatile("" : "+v"(fw), "+v"(fr), "+v"(vo));
  };
  auto final_out = [&]() __attribute__((always_inline)) {
    const u32 fw1 = fw ^ 64, fr1 = fr ^ 16;
    asm volatile("s_nop 3" : "+v"(ap[0][0]), "+v"(ap[1][0]), "+v"(ap[0][1]), "+v"(ap[1][1]));
    *(LAS f32x4*)(lds + fw) = ap[0][0]; *(LAS f32x4*)(lds + fw + 4096) = ap[1][0];
    *(LAS f32x4*)(lds + fw1) = ap[0][1]; *(LAS f32x4*)(lds + fw1 + 4096) = ap[1][1];
    asm volatile("s_nop 1" : "+v"(ap[0][2]), "+v"(ap[1][2]), "+v"(ap[0][3]), "+v"(ap[1][3]));
    *(LAS f32x4*)(lds + fw + 128) = ap[0][2]; *(LAS f32x4*)(lds + fw + 4096 + 128) = ap[1][2];
    *(LAS f32x4*)(lds + fw1 + 128) = ap[0][3]; *(LAS f32x4*)(lds + fw1 + 4096 + 128) = ap[1][3];
    asm volatile("; PINA %0" : "+v"(xres));
    f32x4 x0 = {uf(xres[0] << 16), uf(xres[0] & 0xffff0000u), uf(xres[1] << 16), uf(xres[1] & 0xffff0000u)};
    f32x4 x1 = {uf(xres[2] << 16), uf(xres[2] & 0xffff0000u), uf(xres[3] << 16), uf(xres[3] & 0xffff0000u)};
    const srd_t sOut = mk_srd(out, S_ * XS);
    asm volatile("" : "+v"(x0), "+v"(x1));
    BARRIER();
    f32x4 p[8];
#pragma unroll
    for (int i = 0; i < 4; ++i) { p[i] = *(LAS f32x4*)(lds + fr + i * SLOT); p[4 + i] = *(LAS f32x4*)(lds + fr1 + i * SLOT); }
    SB;
    __builtin_amdgcn_raw_buffer_store_b128(pack8(fix4(x0, (p[0] + p[1]) + (p[2] + p[3]), r2), fix4(x1, (p[4] + p[5]) + (p[6] + p[7]), r2)), sOut, vo, 0, 16);
  };

  SCHED
  if (b == 0 && w == 0) SETFLAG(WS_EPOCH);
  asm volatile("kcode_end:");
}

extern "C" void kernel_launch(void* const* d_in, const int* in_sizes, int n_in, void* d_out, int out_size, void* d_ws, size_t ws_size, hipStream_t stream) {
  (void)in_sizes; (void)n_in; (void)out_size; (void)ws_size;
  (void)hipFuncSetAttribute((const void*)fused_kernel, hipFuncAttributeMaxDynamicSharedMemorySize, 163840);
  fused_kernel<<<256, 512, 163840, stream>>>((const char*)d_in[0], (const char*)d_in[1], (const char*)d_in[2], (const char*)d_in[3],
      (const char*)d_in[5], (const char*)d_in[6], (const char*)d_in[7], (char*)d_out, (char*)d_ws);
}
```
